```python
import torch

def _softmax(x, axis=-1):
    return torch.softmax(x, dim=axis)
def _gelu(x):
    return torch.nn.functional.gelu(x, approximate="tanh")
def _silu(x):
    return torch.nn.functional.silu(x)
def _sigmoid(x):
    return torch.sigmoid(x)
def _layernorm(x, axis=-1, eps=1e-5):
    m = x.mean(dim=axis, keepdim=True)
    v = x.var(dim=axis, keepdim=True, unbiased=False)
    return (x - m) / torch.sqrt(v + eps)
def _scatter_add(tgt, idx, src, axis=0):
    return tgt.clone().index_add_(axis, idx, src)
def _segment_sum(src, seg, n):
    out = torch.zeros((n,) + src.shape[1:], dtype=src.dtype)
    return out.index_add_(0, seg, src)
def _pad(x, pw):
    flat = tuple(v for pair in reversed(pw) for v in pair)
    return torch.nn.functional.pad(x, flat)

kernel_name = 'recurrent_scan__mha_block__moe_arch_mod__prompt'

def setup_inputs():

    _w = (lambda a: (lambda t: t.to(torch.bfloat16) if t.is_floating_point() else t)(torch.as_tensor(a)))
    def _A_setup():
        B, S, D, H = 1, 2048, 640, 32
        return {
            "x": _w((lambda *s: torch.randn(*s, dtype=torch.bfloat16) * 0.05)(B, S, D)),
            "wqkv": _w((lambda *s: torch.randn(*s, dtype=torch.bfloat16) * 0.05)(D, 3 * D)),
            "wo": _w((lambda *s: torch.randn(*s, dtype=torch.bfloat16) * 0.05)(D, D)),
        }
    _A_in = _A_setup()

    def _B_setup():
        B, S, D, F, C = 1, 2048, 640, 1536, 8
        return {
            "x": _w((lambda *s: torch.randn(*s, dtype=torch.bfloat16) * 0.05)(B, S, D)),
            "w_r": _w((lambda *s: torch.randn(*s, dtype=torch.bfloat16) * 0.05)(D)),
            "w_gate": _w((lambda *s: torch.randn(*s, dtype=torch.bfloat16) * 0.05)(D, F)),
            "w_up": _w((lambda *s: torch.randn(*s, dtype=torch.bfloat16) * 0.05)(D, F)),
            "w_down": _w((lambda *s: torch.randn(*s, dtype=torch.bfloat16) * 0.05)(F, D)),
        }
    _B_in = _B_setup()

    return {
        "A_x": _A_in["x"],
        "A_wqkv": _A_in["wqkv"],
        "A_wo": _A_in["wo"],
        "B_x": _B_in["x"],
        "B_w_r": _B_in["w_r"],
        "B_w_gate": _B_in["w_gate"],
        "B_w_up": _B_in["w_up"],
        "B_w_down": _B_in["w_down"],
    }

def reference(A_x, A_wqkv, A_wo, B_x, B_w_r, B_w_gate, B_w_up, B_w_down):
    def _A_ref(x, wqkv, wo):
        B, S, D = x.shape
        H = 32
        Dh = D // H
        qkv = (lambda x, *d: x.permute(*d))((x @ wqkv).reshape(B, S, 3, H, Dh), 2, 0, 3, 1, 4)
        q, k, v = qkv[0], qkv[1], qkv[2]
        scores = torch.einsum("bhqd,bhkd->bhqk", q, k) * (Dh ** -0.5)
        p = _softmax(scores, axis=-1)
        o = torch.einsum("bhqk,bhkd->bhqd", p, v)
        return (lambda x, *d: x.permute(*d))(o, 0, 2, 1, 3).reshape(B, S, D) @ wo

    def _B_ref(x, w_r, w_gate, w_up, w_down):
        B, S, D = x.shape
        C = 8
        r = x @ w_r
        idx = torch.argsort(-r, axis=-1)[:, :C]
        idx_e = (lambda x, n, axis: torch.repeat_interleave(x, n, dim=axis))(idx.reshape(B, C, 1), D, axis=2)
        x_sel = (lambda x, i, axis: torch.take_along_dim(x, i, dim=axis))(x, idx_e, axis=1)
        g = _silu(x_sel @ w_gate)
        u = x_sel @ w_up
        y = (g * u) @ w_down
        r_sel = _sigmoid((lambda x, i, axis: torch.take_along_dim(x, i, dim=axis))(r, idx, axis=1))
        y = y * r_sel.reshape(B, C, 1)
        b_off = (torch.arange(B) * S).reshape(B, 1)
        flat_idx = (idx + b_off).reshape(B * C)
        zero = (x * 0.0).reshape(B * S, D)
        upd = _scatter_add(zero, flat_idx, y.reshape(B * C, D), axis=0)
        return x + upd.reshape(B, S, D)

    x = A_x
    xn = _layernorm(x, axis=-1)
    s = _A_ref(xn, A_wqkv, A_wo)
    g = _silu(_B_ref(xn, B_w_r, B_w_gate, B_w_up, B_w_down))
    return x + s * g
```

```cpp
#include <hip/hip_runtime.h>
#include <stdint.h>

typedef __attribute__((ext_vector_type(8))) short bf16x8;
typedef __attribute__((ext_vector_type(4))) short bf16x4;
typedef __attribute__((ext_vector_type(16))) float f32x16;
typedef __attribute__((ext_vector_type(4))) float f32x4;
typedef __attribute__((ext_vector_type(32))) unsigned u32x32;
typedef __attribute__((ext_vector_type(16))) unsigned u32x16;
typedef __attribute__((ext_vector_type(4))) unsigned u32x4;
typedef __attribute__((ext_vector_type(2))) unsigned u32x2;
typedef __amdgpu_buffer_rsrc_t rsrc_t;
#define LAS __attribute__((address_space(3)))
#define DI __device__ __forceinline__

#define ATTN_ZERO_C \
  "v_mov_b32 v64, 0\n" \
  "v_mov_b32 v65, 0\n" \
  "v_mov_b32 v66, 0\n" \
  "v_mov_b32 v67, 0\n" \
  "v_mov_b32 v68, 0\n" \
  "v_mov_b32 v69, 0\n" \
  "v_mov_b32 v70, 0\n" \
  "v_mov_b32 v71, 0\n" \
  "v_mov_b32 v72, 0\n" \
  "v_mov_b32 v73, 0\n" \
  "v_mov_b32 v74, 0\n" \
  "v_mov_b32 v75, 0\n" \
  "v_mov_b32 v76, 0\n" \
  "v_mov_b32 v77, 0\n" \
  "v_mov_b32 v78, 0\n" \
  "v_mov_b32 v79, 0\n" \
  "v_mov_b32 v80, 0\n" \
  "v_mov_b32 v81, 0\n" \
  "v_mov_b32 v82, 0\n" \
  "v_mov_b32 v83, 0\n" \
  "v_mov_b32 v84, 0\n" \
  "v_mov_b32 v85, 0\n" \
  "v_mov_b32 v86, 0\n" \
  "v_mov_b32 v87, 0\n" \
  "v_mov_b32 v88, 0\n" \
  "v_mov_b32 v89, 0\n" \
  "v_mov_b32 v90, 0\n" \
  "v_mov_b32 v91, 0\n" \
  "v_mov_b32 v92, 0\n" \
  "v_mov_b32 v93, 0\n" \
  "v_mov_b32 v94, 0\n" \
  "v_mov_b32 v95, 0\n" \
  "v_mov_b32 v96, 0\n" \
  "v_mov_b32 v97, 0\n" \
  "v_mov_b32 v98, 0\n" \
  "v_mov_b32 v99, 0\n" \
  "v_mov_b32 v100, 0\n" \
  "v_mov_b32 v101, 0\n" \
  "v_mov_b32 v102, 0\n" \
  "v_mov_b32 v103, 0\n" \
  "v_mov_b32 v104, 0\n" \
  "v_mov_b32 v105, 0\n" \
  "v_mov_b32 v106, 0\n" \
  "v_mov_b32 v107, 0\n" \
  "v_mov_b32 v108, 0\n" \
  "v_mov_b32 v109, 0\n" \
  "v_mov_b32 v110, 0\n" \
  "v_mov_b32 v111, 0\n" \
  "v_mov_b32 v112, 0\n" \
  "v_mov_b32 v113, 0\n" \
  "v_mov_b32 v114, 0\n" \
  "v_mov_b32 v115, 0\n" \
  "v_mov_b32 v116, 0\n" \
  "v_mov_b32 v117, 0\n" \
  "v_mov_b32 v118, 0\n" \
  "v_mov_b32 v119, 0\n" \
  "v_mov_b32 v120, 0\n" \
  "v_mov_b32 v121, 0\n" \
  "v_mov_b32 v122, 0\n" \
  "v_mov_b32 v123, 0\n" \
  "v_mov_b32 v124, 0\n" \
  "v_mov_b32 v125, 0\n" \
  "v_mov_b32 v126, 0\n" \
  "v_mov_b32 v127, 0\n" \
  "v_mov_b32 v132, 0\n" \
  "v_mov_b32 v133, 0\n" \
  "v_mov_b32 v134, 0\n" \
  "v_mov_b32 v135, 0\n" \
  "v_mov_b32 v144, 0\n" \
  "v_mov_b32 v145, 0\n" \
  "v_mov_b32 v146, 0\n" \
  "v_mov_b32 v147, 0\n" \
  ""
#define ATTN_ASM_C \
  "ds_read_b128 v[156:159], v185 offset:0\n" \
  "ds_read_b128 v[148:151], v184 offset:0\n" \
  "ds_read_b128 v[152:155], v184 offset:1024\n" \
  "ds_read_b128 v[136:139], v186 offset:0\n" \
  "ds_read_b128 v[140:143], v186 offset:672\n" \
  "v_mov_b32 v189, -1\n" \
  "s_waitcnt lgkmcnt(3)\n" \
  "v_mfma_f32_32x32x4_2b_bf16 v[0:31], v[156:157], v[180:181], 0\n" \
  "s_nop 15\n" \
  "s_nop 1\n" \
  "v_mfma_f32_32x32x16_bf16 v[0:15], v[148:151], v[164:167], v[0:15]\n" \
  "v_mfma_f32_32x32x16_bf16 v[16:31], v[148:151], v[168:171], v[16:31]\n" \
  "s_nop 10\n" \
  ".Lloop_%=:\n" \
  "v_mfma_f32_32x32x4_2b_bf16 v[32:63], v[156:157], v[182:183], 0\n" \
  "v_exp_f32 v0, v0\n" \
  "v_exp_f32 v1, v1\n" \
  "v_exp_f32 v2, v2\n" \
  "v_exp_f32 v3, v3\n" \
  "v_cvt_pk_bf16_f32 v128, v0, v1\n" \
  "v_cvt_pk_bf16_f32 v129, v2, v3\n" \
  "v_exp_f32 v4, v4\n" \
  "v_exp_f32 v5, v5\n" \
  "v_exp_f32 v6, v6\n" \
  "v_exp_f32 v7, v7\n" \
  "v_cvt_pk_bf16_f32 v130, v4, v5\n" \
  "v_cvt_pk_bf16_f32 v131, v6, v7\n" \
  "v_mfma_f32_32x32x16_bf16 v[112:127], v[144:147], v[132:135], v[112:127]\n" \
  "v_exp_f32 v16, v16\n" \
  "v_exp_f32 v17, v17\n" \
  "v_exp_f32 v18, v18\n" \
  "v_exp_f32 v19, v19\n" \
  "v_cvt_pk_bf16_f32 v132, v16, v17\n" \
  "v_cvt_pk_bf16_f32 v133, v18, v19\n" \
  "s_waitcnt lgkmcnt(0)\n" \
  "v_mfma_f32_32x32x16_bf16 v[64:79], v[136:139], v[128:131], v[64:79]\n" \
  "v_cmp_gt_u32 vcc, v190, v189\n" \
  "v_exp_f32 v20, v20\n" \
  "v_exp_f32 v21, v21\n" \
  "v_exp_f32 v22, v22\n" \
  "v_exp_f32 v23, v23\n" \
  "v_cvt_pk_bf16_f32 v134, v20, v21\n" \
  "v_cvt_pk_bf16_f32 v135, v22, v23\n" \
  "v_mfma_f32_32x32x16_bf16 v[32:47], v[148:151], v[172:175], v[32:47]\n" \
  "s_cbranch_vccnz .Lspin0_%=\n" \
  ".Lok0_%=:\n" \
  "v_exp_f32 v8, v8\n" \
  "v_exp_f32 v9, v9\n" \
  "v_exp_f32 v10, v10\n" \
  "v_exp_f32 v11, v11\n" \
  "v_cvt_pk_bf16_f32 v128, v8, v9\n" \
  "v_cvt_pk_bf16_f32 v129, v10, v11\n" \
  "v_mfma_f32_32x32x16_bf16 v[80:95], v[136:139], v[132:135], v[80:95]\n" \
  "v_exp_f32 v12, v12\n" \
  "v_exp_f32 v13, v13\n" \
  "v_exp_f32 v14, v14\n" \
  "v_exp_f32 v15, v15\n" \
  "v_cvt_pk_bf16_f32 v130, v12, v13\n" \
  "v_cvt_pk_bf16_f32 v131, v14, v15\n" \
  "v_mfma_f32_32x32x16_bf16 v[48:63], v[148:151], v[176:179], v[48:63]\n" \
  "v_exp_f32 v24, v24\n" \
  "v_exp_f32 v25, v25\n" \
  "v_exp_f32 v26, v26\n" \
  "v_exp_f32 v27, v27\n" \
  "v_cvt_pk_bf16_f32 v132, v24, v25\n" \
  "v_cvt_pk_bf16_f32 v133, v26, v27\n" \
  "v_mfma_f32_32x32x16_bf16 v[64:79], v[140:143], v[128:131], v[64:79]\n" \
  "v_exp_f32 v28, v28\n" \
  "v_exp_f32 v29, v29\n" \
  "v_exp_f32 v30, v30\n" \
  "v_exp_f32 v31, v31\n" \
  "v_cvt_pk_bf16_f32 v134, v28, v29\n" \
  "v_cvt_pk_bf16_f32 v135, v30, v31\n" \
  "v_mfma_f32_32x32x4_2b_bf16 v[0:31], v[158:159], v[180:181], 0\n" \
  "v_exp_f32 v32, v32\n" \
  "v_exp_f32 v33, v33\n" \
  "v_exp_f32 v34, v34\n" \
  "v_exp_f32 v35, v35\n" \
  "v_cvt_pk_bf16_f32 v128, v32, v33\n" \
  "v_cvt_pk_bf16_f32 v129, v34, v35\n" \
  "v_exp_f32 v36, v36\n" \
  "v_exp_f32 v37, v37\n" \
  "v_exp_f32 v38, v38\n" \
  "v_exp_f32 v39, v39\n" \
  "v_cvt_pk_bf16_f32 v130, v36, v37\n" \
  "v_cvt_pk_bf16_f32 v131, v38, v39\n" \
  "v_mfma_f32_32x32x16_bf16 v[80:95], v[140:143], v[132:135], v[80:95]\n" \
  "v_exp_f32 v48, v48\n" \
  "v_exp_f32 v49, v49\n" \
  "v_exp_f32 v50, v50\n" \
  "v_exp_f32 v51, v51\n" \
  "v_cvt_pk_bf16_f32 v132, v48, v49\n" \
  "v_cvt_pk_bf16_f32 v133, v50, v51\n" \
  "v_mfma_f32_32x32x16_bf16 v[96:111], v[136:139], v[128:131], v[96:111]\n" \
  "v_exp_f32 v52, v52\n" \
  "v_exp_f32 v53, v53\n" \
  "v_exp_f32 v54, v54\n" \
  "v_exp_f32 v55, v55\n" \
  "v_cvt_pk_bf16_f32 v134, v52, v53\n" \
  "v_cvt_pk_bf16_f32 v135, v54, v55\n" \
  "v_mfma_f32_32x32x16_bf16 v[0:15], v[152:155], v[164:167], v[0:15]\n" \
  "v_exp_f32 v40, v40\n" \
  "v_exp_f32 v41, v41\n" \
  "v_exp_f32 v42, v42\n" \
  "v_exp_f32 v43, v43\n" \
  "v_cvt_pk_bf16_f32 v128, v40, v41\n" \
  "v_cvt_pk_bf16_f32 v129, v42, v43\n" \
  "v_mfma_f32_32x32x16_bf16 v[112:127], v[136:139], v[132:135], v[112:127]\n" \
  "ds_read_b128 v[136:139], v186 offset:1344\n" \
  "ds_read_b128 v[144:147], v186 offset:2016\n" \
  "v_exp_f32 v44, v44\n" \
  "v_exp_f32 v45, v45\n" \
  "v_exp_f32 v46, v46\n" \
  "v_exp_f32 v47, v47\n" \
  "v_cvt_pk_bf16_f32 v130, v44, v45\n" \
  "v_cvt_pk_bf16_f32 v131, v46, v47\n" \
  "v_mfma_f32_32x32x16_bf16 v[16:31], v[152:155], v[168:171], v[16:31]\n" \
  "ds_read_b128 v[148:151], v184 offset:10496\n" \
  "ds_read_b128 v[160:163], v185 offset:10496\n" \
  "v_exp_f32 v56, v56\n" \
  "v_exp_f32 v57, v57\n" \
  "v_exp_f32 v58, v58\n" \
  "v_exp_f32 v59, v59\n" \
  "v_cvt_pk_bf16_f32 v132, v56, v57\n" \
  "v_cvt_pk_bf16_f32 v133, v58, v59\n" \
  "v_mfma_f32_32x32x16_bf16 v[96:111], v[140:143], v[128:131], v[96:111]\n" \
  "v_exp_f32 v60, v60\n" \
  "v_exp_f32 v61, v61\n" \
  "v_exp_f32 v62, v62\n" \
  "v_exp_f32 v63, v63\n" \
  "v_cvt_pk_bf16_f32 v134, v60, v61\n" \
  "v_cvt_pk_bf16_f32 v135, v62, v63\n" \
  "v_mfma_f32_32x32x4_2b_bf16 v[32:63], v[158:159], v[182:183], 0\n" \
  "v_exp_f32 v0, v0\n" \
  "v_exp_f32 v1, v1\n" \
  "v_exp_f32 v2, v2\n" \
  "v_exp_f32 v3, v3\n" \
  "v_cvt_pk_bf16_f32 v128, v0, v1\n" \
  "v_cvt_pk_bf16_f32 v129, v2, v3\n" \
  "v_exp_f32 v4, v4\n" \
  "v_exp_f32 v5, v5\n" \
  "v_exp_f32 v6, v6\n" \
  "v_exp_f32 v7, v7\n" \
  "v_cvt_pk_bf16_f32 v130, v4, v5\n" \
  "v_cvt_pk_bf16_f32 v131, v6, v7\n" \
  "v_mfma_f32_32x32x16_bf16 v[112:127], v[140:143], v[132:135], v[112:127]\n" \
  "v_exp_f32 v16, v16\n" \
  "v_exp_f32 v17, v17\n" \
  "v_exp_f32 v18, v18\n" \
  "v_exp_f32 v19, v19\n" \
  "v_cvt_pk_bf16_f32 v132, v16, v17\n" \
  "v_cvt_pk_bf16_f32 v133, v18, v19\n" \
  "s_waitcnt lgkmcnt(0)\n" \
  "v_mfma_f32_32x32x16_bf16 v[64:79], v[136:139], v[128:131], v[64:79]\n" \
  "ds_add_rtn_u32 v189, v187, v188 offset:0\n" \
  "v_exp_f32 v20, v20\n" \
  "v_exp_f32 v21, v21\n" \
  "v_exp_f32 v22, v22\n" \
  "v_exp_f32 v23, v23\n" \
  "v_cvt_pk_bf16_f32 v134, v20, v21\n" \
  "v_cvt_pk_bf16_f32 v135, v22, v23\n" \
  "v_mfma_f32_32x32x16_bf16 v[32:47], v[152:155], v[172:175], v[32:47]\n" \
  "v_exp_f32 v8, v8\n" \
  "v_exp_f32 v9, v9\n" \
  "v_exp_f32 v10, v10\n" \
  "v_exp_f32 v11, v11\n" \
  "v_cvt_pk_bf16_f32 v128, v8, v9\n" \
  "v_cvt_pk_bf16_f32 v129, v10, v11\n" \
  "v_mfma_f32_32x32x16_bf16 v[80:95], v[136:139], v[132:135], v[80:95]\n" \
  "v_exp_f32 v12, v12\n" \
  "v_exp_f32 v13, v13\n" \
  "v_exp_f32 v14, v14\n" \
  "v_exp_f32 v15, v15\n" \
  "v_cvt_pk_bf16_f32 v130, v12, v13\n" \
  "v_cvt_pk_bf16_f32 v131, v14, v15\n" \
  "v_mfma_f32_32x32x16_bf16 v[48:63], v[152:155], v[176:179], v[48:63]\n" \
  "v_exp_f32 v24, v24\n" \
  "v_exp_f32 v25, v25\n" \
  "v_exp_f32 v26, v26\n" \
  "v_exp_f32 v27, v27\n" \
  "v_cvt_pk_bf16_f32 v132, v24, v25\n" \
  "v_cvt_pk_bf16_f32 v133, v26, v27\n" \
  "v_mfma_f32_32x32x16_bf16 v[64:79], v[144:147], v[128:131], v[64:79]\n" \
  "v_exp_f32 v28, v28\n" \
  "v_exp_f32 v29, v29\n" \
  "v_exp_f32 v30, v30\n" \
  "v_exp_f32 v31, v31\n" \
  "v_cvt_pk_bf16_f32 v134, v28, v29\n" \
  "v_cvt_pk_bf16_f32 v135, v30, v31\n" \
  "v_mfma_f32_32x32x4_2b_bf16 v[0:31], v[160:161], v[180:181], 0\n" \
  "v_exp_f32 v32, v32\n" \
  "v_exp_f32 v33, v33\n" \
  "v_exp_f32 v34, v34\n" \
  "v_exp_f32 v35, v35\n" \
  "v_cvt_pk_bf16_f32 v128, v32, v33\n" \
  "v_cvt_pk_bf16_f32 v129, v34, v35\n" \
  "v_exp_f32 v36, v36\n" \
  "v_exp_f32 v37, v37\n" \
  "v_exp_f32 v38, v38\n" \
  "v_exp_f32 v39, v39\n" \
  "v_cvt_pk_bf16_f32 v130, v36, v37\n" \
  "v_cvt_pk_bf16_f32 v131, v38, v39\n" \
  "v_mfma_f32_32x32x16_bf16 v[80:95], v[144:147], v[132:135], v[80:95]\n" \
  "v_exp_f32 v48, v48\n" \
  "v_exp_f32 v49, v49\n" \
  "v_exp_f32 v50, v50\n" \
  "v_exp_f32 v51, v51\n" \
  "v_cvt_pk_bf16_f32 v132, v48, v49\n" \
  "v_cvt_pk_bf16_f32 v133, v50, v51\n" \
  "v_mfma_f32_32x32x16_bf16 v[96:111], v[136:139], v[128:131], v[96:111]\n" \
  "v_exp_f32 v52, v52\n" \
  "v_exp_f32 v53, v53\n" \
  "v_exp_f32 v54, v54\n" \
  "v_exp_f32 v55, v55\n" \
  "v_cvt_pk_bf16_f32 v134, v52, v53\n" \
  "v_cvt_pk_bf16_f32 v135, v54, v55\n" \
  "v_mfma_f32_32x32x16_bf16 v[0:15], v[148:151], v[164:167], v[0:15]\n" \
  "v_exp_f32 v40, v40\n" \
  "v_exp_f32 v41, v41\n" \
  "v_exp_f32 v42, v42\n" \
  "v_exp_f32 v43, v43\n" \
  "v_cvt_pk_bf16_f32 v128, v40, v41\n" \
  "v_cvt_pk_bf16_f32 v129, v42, v43\n" \
  "v_mfma_f32_32x32x16_bf16 v[112:127], v[136:139], v[132:135], v[112:127]\n" \
  "ds_read_b128 v[136:139], v186 offset:10496\n" \
  "ds_read_b128 v[140:143], v186 offset:11168\n" \
  "v_exp_f32 v44, v44\n" \
  "v_exp_f32 v45, v45\n" \
  "v_exp_f32 v46, v46\n" \
  "v_exp_f32 v47, v47\n" \
  "v_cvt_pk_bf16_f32 v130, v44, v45\n" \
  "v_cvt_pk_bf16_f32 v131, v46, v47\n" \
  "v_mfma_f32_32x32x16_bf16 v[16:31], v[148:151], v[168:171], v[16:31]\n" \
  "ds_read_b128 v[152:155], v184 offset:11520\n" \
  "v_exp_f32 v56, v56\n" \
  "v_exp_f32 v57, v57\n" \
  "v_exp_f32 v58, v58\n" \
  "v_exp_f32 v59, v59\n" \
  "v_cvt_pk_bf16_f32 v132, v56, v57\n" \
  "v_cvt_pk_bf16_f32 v133, v58, v59\n" \
  "v_mfma_f32_32x32x16_bf16 v[96:111], v[144:147], v[128:131], v[96:111]\n" \
  "v_exp_f32 v60, v60\n" \
  "v_exp_f32 v61, v61\n" \
  "v_exp_f32 v62, v62\n" \
  "v_exp_f32 v63, v63\n" \
  "v_cvt_pk_bf16_f32 v134, v60, v61\n" \
  "v_cvt_pk_bf16_f32 v135, v62, v63\n" \
  "v_mfma_f32_32x32x4_2b_bf16 v[32:63], v[160:161], v[182:183], 0\n" \
  "v_exp_f32 v0, v0\n" \
  "v_exp_f32 v1, v1\n" \
  "v_exp_f32 v2, v2\n" \
  "v_exp_f32 v3, v3\n" \
  "v_cvt_pk_bf16_f32 v128, v0, v1\n" \
  "v_cvt_pk_bf16_f32 v129, v2, v3\n" \
  "v_exp_f32 v4, v4\n" \
  "v_exp_f32 v5, v5\n" \
  "v_exp_f32 v6, v6\n" \
  "v_exp_f32 v7, v7\n" \
  "v_cvt_pk_bf16_f32 v130, v4, v5\n" \
  "v_cvt_pk_bf16_f32 v131, v6, v7\n" \
  "v_mfma_f32_32x32x16_bf16 v[112:127], v[144:147], v[132:135], v[112:127]\n" \
  "v_exp_f32 v16, v16\n" \
  "v_exp_f32 v17, v17\n" \
  "v_exp_f32 v18, v18\n" \
  "v_exp_f32 v19, v19\n" \
  "v_cvt_pk_bf16_f32 v132, v16, v17\n" \
  "v_cvt_pk_bf16_f32 v133, v18, v19\n" \
  "s_waitcnt lgkmcnt(0)\n" \
  "v_mfma_f32_32x32x16_bf16 v[64:79], v[136:139], v[128:131], v[64:79]\n" \
  "v_cmp_gt_u32 vcc, v190, v189\n" \
  "v_exp_f32 v20, v20\n" \
  "v_exp_f32 v21, v21\n" \
  "v_exp_f32 v22, v22\n" \
  "v_exp_f32 v23, v23\n" \
  "v_cvt_pk_bf16_f32 v134, v20, v21\n" \
  "v_cvt_pk_bf16_f32 v135, v22, v23\n" \
  "v_mfma_f32_32x32x16_bf16 v[32:47], v[148:151], v[172:175], v[32:47]\n" \
  "s_cbranch_vccnz .Lspin4_%=\n" \
  ".Lok4_%=:\n" \
  "v_exp_f32 v8, v8\n" \
  "v_exp_f32 v9, v9\n" \
  "v_exp_f32 v10, v10\n" \
  "v_exp_f32 v11, v11\n" \
  "v_cvt_pk_bf16_f32 v128, v8, v9\n" \
  "v_cvt_pk_bf16_f32 v129, v10, v11\n" \
  "v_mfma_f32_32x32x16_bf16 v[80:95], v[136:139], v[132:135], v[80:95]\n" \
  "v_exp_f32 v12, v12\n" \
  "v_exp_f32 v13, v13\n" \
  "v_exp_f32 v14, v14\n" \
  "v_exp_f32 v15, v15\n" \
  "v_cvt_pk_bf16_f32 v130, v12, v13\n" \
  "v_cvt_pk_bf16_f32 v131, v14, v15\n" \
  "v_mfma_f32_32x32x16_bf16 v[48:63], v[148:151], v[176:179], v[48:63]\n" \
  "v_exp_f32 v24, v24\n" \
  "v_exp_f32 v25, v25\n" \
  "v_exp_f32 v26, v26\n" \
  "v_exp_f32 v27, v27\n" \
  "v_cvt_pk_bf16_f32 v132, v24, v25\n" \
  "v_cvt_pk_bf16_f32 v133, v26, v27\n" \
  "v_mfma_f32_32x32x16_bf16 v[64:79], v[140:143], v[128:131], v[64:79]\n" \
  "v_exp_f32 v28, v28\n" \
  "v_exp_f32 v29, v29\n" \
  "v_exp_f32 v30, v30\n" \
  "v_exp_f32 v31, v31\n" \
  "v_cvt_pk_bf16_f32 v134, v28, v29\n" \
  "v_cvt_pk_bf16_f32 v135, v30, v31\n" \
  "v_mfma_f32_32x32x4_2b_bf16 v[0:31], v[162:163], v[180:181], 0\n" \
  "v_exp_f32 v32, v32\n" \
  "v_exp_f32 v33, v33\n" \
  "v_exp_f32 v34, v34\n" \
  "v_exp_f32 v35, v35\n" \
  "v_cvt_pk_bf16_f32 v128, v32, v33\n" \
  "v_cvt_pk_bf16_f32 v129, v34, v35\n" \
  "v_exp_f32 v36, v36\n" \
  "v_exp_f32 v37, v37\n" \
  "v_exp_f32 v38, v38\n" \
  "v_exp_f32 v39, v39\n" \
  "v_cvt_pk_bf16_f32 v130, v36, v37\n" \
  "v_cvt_pk_bf16_f32 v131, v38, v39\n" \
  "v_mfma_f32_32x32x16_bf16 v[80:95], v[140:143], v[132:135], v[80:95]\n" \
  "v_exp_f32 v48, v48\n" \
  "v_exp_f32 v49, v49\n" \
  "v_exp_f32 v50, v50\n" \
  "v_exp_f32 v51, v51\n" \
  "v_cvt_pk_bf16_f32 v132, v48, v49\n" \
  "v_cvt_pk_bf16_f32 v133, v50, v51\n" \
  "v_mfma_f32_32x32x16_bf16 v[96:111], v[136:139], v[128:131], v[96:111]\n" \
  "v_exp_f32 v52, v52\n" \
  "v_exp_f32 v53, v53\n" \
  "v_exp_f32 v54, v54\n" \
  "v_exp_f32 v55, v55\n" \
  "v_cvt_pk_bf16_f32 v134, v52, v53\n" \
  "v_cvt_pk_bf16_f32 v135, v54, v55\n" \
  "v_mfma_f32_32x32x16_bf16 v[0:15], v[152:155], v[164:167], v[0:15]\n" \
  "v_exp_f32 v40, v40\n" \
  "v_exp_f32 v41, v41\n" \
  "v_exp_f32 v42, v42\n" \
  "v_exp_f32 v43, v43\n" \
  "v_cvt_pk_bf16_f32 v128, v40, v41\n" \
  "v_cvt_pk_bf16_f32 v129, v42, v43\n" \
  "v_mfma_f32_32x32x16_bf16 v[112:127], v[136:139], v[132:135], v[112:127]\n" \
  "ds_read_b128 v[136:139], v186 offset:11840\n" \
  "ds_read_b128 v[144:147], v186 offset:12512\n" \
  "v_exp_f32 v44, v44\n" \
  "v_exp_f32 v45, v45\n" \
  "v_exp_f32 v46, v46\n" \
  "v_exp_f32 v47, v47\n" \
  "v_cvt_pk_bf16_f32 v130, v44, v45\n" \
  "v_cvt_pk_bf16_f32 v131, v46, v47\n" \
  "v_mfma_f32_32x32x16_bf16 v[16:31], v[152:155], v[168:171], v[16:31]\n" \
  "ds_read_b128 v[148:151], v184 offset:20992\n" \
  "ds_read_b128 v[156:159], v185 offset:20992\n" \
  "v_exp_f32 v56, v56\n" \
  "v_exp_f32 v57, v57\n" \
  "v_exp_f32 v58, v58\n" \
  "v_exp_f32 v59, v59\n" \
  "v_cvt_pk_bf16_f32 v132, v56, v57\n" \
  "v_cvt_pk_bf16_f32 v133, v58, v59\n" \
  "v_mfma_f32_32x32x16_bf16 v[96:111], v[140:143], v[128:131], v[96:111]\n" \
  "v_exp_f32 v60, v60\n" \
  "v_exp_f32 v61, v61\n" \
  "v_exp_f32 v62, v62\n" \
  "v_exp_f32 v63, v63\n" \
  "v_cvt_pk_bf16_f32 v134, v60, v61\n" \
  "v_cvt_pk_bf16_f32 v135, v62, v63\n" \
  "v_mfma_f32_32x32x4_2b_bf16 v[32:63], v[162:163], v[182:183], 0\n" \
  "v_exp_f32 v0, v0\n" \
  "v_exp_f32 v1, v1\n" \
  "v_exp_f32 v2, v2\n" \
  "v_exp_f32 v3, v3\n" \
  "v_cvt_pk_bf16_f32 v128, v0, v1\n" \
  "v_cvt_pk_bf16_f32 v129, v2, v3\n" \
  "v_exp_f32 v4, v4\n" \
  "v_exp_f32 v5, v5\n" \
  "v_exp_f32 v6, v6\n" \
  "v_exp_f32 v7, v7\n" \
  "v_cvt_pk_bf16_f32 v130, v4, v5\n" \
  "v_cvt_pk_bf16_f32 v131, v6, v7\n" \
  "v_mfma_f32_32x32x16_bf16 v[112:127], v[140:143], v[132:135], v[112:127]\n" \
  "v_exp_f32 v16, v16\n" \
  "v_exp_f32 v17, v17\n" \
  "v_exp_f32 v18, v18\n" \
  "v_exp_f32 v19, v19\n" \
  "v_cvt_pk_bf16_f32 v132, v16, v17\n" \
  "v_cvt_pk_bf16_f32 v133, v18, v19\n" \
  "s_waitcnt lgkmcnt(0)\n" \
  "v_mfma_f32_32x32x16_bf16 v[64:79], v[136:139], v[128:131], v[64:79]\n" \
  "ds_add_rtn_u32 v189, v187, v188 offset:4\n" \
  "v_exp_f32 v20, v20\n" \
  "v_exp_f32 v21, v21\n" \
  "v_exp_f32 v22, v22\n" \
  "v_exp_f32 v23, v23\n" \
  "v_cvt_pk_bf16_f32 v134, v20, v21\n" \
  "v_cvt_pk_bf16_f32 v135, v22, v23\n" \
  "v_mfma_f32_32x32x16_bf16 v[32:47], v[152:155], v[172:175], v[32:47]\n" \
  "v_exp_f32 v8, v8\n" \
  "v_exp_f32 v9, v9\n" \
  "v_exp_f32 v10, v10\n" \
  "v_exp_f32 v11, v11\n" \
  "v_cvt_pk_bf16_f32 v128, v8, v9\n" \
  "v_cvt_pk_bf16_f32 v129, v10, v11\n" \
  "v_mfma_f32_32x32x16_bf16 v[80:95], v[136:139], v[132:135], v[80:95]\n" \
  "v_exp_f32 v12, v12\n" \
  "v_exp_f32 v13, v13\n" \
  "v_exp_f32 v14, v14\n" \
  "v_exp_f32 v15, v15\n" \
  "v_cvt_pk_bf16_f32 v130, v12, v13\n" \
  "v_cvt_pk_bf16_f32 v131, v14, v15\n" \
  "v_mfma_f32_32x32x16_bf16 v[48:63], v[152:155], v[176:179], v[48:63]\n" \
  "v_exp_f32 v24, v24\n" \
  "v_exp_f32 v25, v25\n" \
  "v_exp_f32 v26, v26\n" \
  "v_exp_f32 v27, v27\n" \
  "v_cvt_pk_bf16_f32 v132, v24, v25\n" \
  "v_cvt_pk_bf16_f32 v133, v26, v27\n" \
  "v_mfma_f32_32x32x16_bf16 v[64:79], v[144:147], v[128:131], v[64:79]\n" \
  "v_exp_f32 v28, v28\n" \
  "v_exp_f32 v29, v29\n" \
  "v_exp_f32 v30, v30\n" \
  "v_exp_f32 v31, v31\n" \
  "v_cvt_pk_bf16_f32 v134, v28, v29\n" \
  "v_cvt_pk_bf16_f32 v135, v30, v31\n" \
  "v_mfma_f32_32x32x4_2b_bf16 v[0:31], v[156:157], v[180:181], 0\n" \
  "v_exp_f32 v32, v32\n" \
  "v_exp_f32 v33, v33\n" \
  "v_exp_f32 v34, v34\n" \
  "v_exp_f32 v35, v35\n" \
  "v_cvt_pk_bf16_f32 v128, v32, v33\n" \
  "v_cvt_pk_bf16_f32 v129, v34, v35\n" \
  "v_exp_f32 v36, v36\n" \
  "v_exp_f32 v37, v37\n" \
  "v_exp_f32 v38, v38\n" \
  "v_exp_f32 v39, v39\n" \
  "v_cvt_pk_bf16_f32 v130, v36, v37\n" \
  "v_cvt_pk_bf16_f32 v131, v38, v39\n" \
  "v_mfma_f32_32x32x16_bf16 v[80:95], v[144:147], v[132:135], v[80:95]\n" \
  "v_exp_f32 v48, v48\n" \
  "v_exp_f32 v49, v49\n" \
  "v_exp_f32 v50, v50\n" \
  "v_exp_f32 v51, v51\n" \
  "v_cvt_pk_bf16_f32 v132, v48, v49\n" \
  "v_cvt_pk_bf16_f32 v133, v50, v51\n" \
  "v_mfma_f32_32x32x16_bf16 v[96:111], v[136:139], v[128:131], v[96:111]\n" \
  "v_exp_f32 v52, v52\n" \
  "v_exp_f32 v53, v53\n" \
  "v_exp_f32 v54, v54\n" \
  "v_exp_f32 v55, v55\n" \
  "v_cvt_pk_bf16_f32 v134, v52, v53\n" \
  "v_cvt_pk_bf16_f32 v135, v54, v55\n" \
  "v_mfma_f32_32x32x16_bf16 v[0:15], v[148:151], v[164:167], v[0:15]\n" \
  "v_exp_f32 v40, v40\n" \
  "v_exp_f32 v41, v41\n" \
  "v_exp_f32 v42, v42\n" \
  "v_exp_f32 v43, v43\n" \
  "v_cvt_pk_bf16_f32 v128, v40, v41\n" \
  "v_cvt_pk_bf16_f32 v129, v42, v43\n" \
  "v_mfma_f32_32x32x16_bf16 v[112:127], v[136:139], v[132:135], v[112:127]\n" \
  "ds_read_b128 v[136:139], v186 offset:20992\n" \
  "ds_read_b128 v[140:143], v186 offset:21664\n" \
  "v_exp_f32 v44, v44\n" \
  "v_exp_f32 v45, v45\n" \
  "v_exp_f32 v46, v46\n" \
  "v_exp_f32 v47, v47\n" \
  "v_cvt_pk_bf16_f32 v130, v44, v45\n" \
  "v_cvt_pk_bf16_f32 v131, v46, v47\n" \
  "v_mfma_f32_32x32x16_bf16 v[16:31], v[148:151], v[168:171], v[16:31]\n" \
  "ds_read_b128 v[152:155], v184 offset:22016\n" \
  "v_exp_f32 v56, v56\n" \
  "v_exp_f32 v57, v57\n" \
  "v_exp_f32 v58, v58\n" \
  "v_exp_f32 v59, v59\n" \
  "v_cvt_pk_bf16_f32 v132, v56, v57\n" \
  "v_cvt_pk_bf16_f32 v133, v58, v59\n" \
  "v_mfma_f32_32x32x16_bf16 v[96:111], v[144:147], v[128:131], v[96:111]\n" \
  "v_exp_f32 v60, v60\n" \
  "v_exp_f32 v61, v61\n" \
  "v_exp_f32 v62, v62\n" \
  "v_exp_f32 v63, v63\n" \
  "v_cvt_pk_bf16_f32 v134, v60, v61\n" \
  "v_cvt_pk_bf16_f32 v135, v62, v63\n" \
  "v_mfma_f32_32x32x4_2b_bf16 v[32:63], v[156:157], v[182:183], 0\n" \
  "v_exp_f32 v0, v0\n" \
  "v_exp_f32 v1, v1\n" \
  "v_exp_f32 v2, v2\n" \
  "v_exp_f32 v3, v3\n" \
  "v_cvt_pk_bf16_f32 v128, v0, v1\n" \
  "v_cvt_pk_bf16_f32 v129, v2, v3\n" \
  "v_exp_f32 v4, v4\n" \
  "v_exp_f32 v5, v5\n" \
  "v_exp_f32 v6, v6\n" \
  "v_exp_f32 v7, v7\n" \
  "v_cvt_pk_bf16_f32 v130, v4, v5\n" \
  "v_cvt_pk_bf16_f32 v131, v6, v7\n" \
  "v_mfma_f32_32x32x16_bf16 v[112:127], v[144:147], v[132:135], v[112:127]\n" \
  "v_exp_f32 v16, v16\n" \
  "v_exp_f32 v17, v17\n" \
  "v_exp_f32 v18, v18\n" \
  "v_exp_f32 v19, v19\n" \
  "v_cvt_pk_bf16_f32 v132, v16, v17\n" \
  "v_cvt_pk_bf16_f32 v133, v18, v19\n" \
  "s_waitcnt lgkmcnt(0)\n" \
  "v_mfma_f32_32x32x16_bf16 v[64:79], v[136:139], v[128:131], v[64:79]\n" \
  "v_cmp_gt_u32 vcc, v190, v189\n" \
  "v_exp_f32 v20, v20\n" \
  "v_exp_f32 v21, v21\n" \
  "v_exp_f32 v22, v22\n" \
  "v_exp_f32 v23, v23\n" \
  "v_cvt_pk_bf16_f32 v134, v20, v21\n" \
  "v_cvt_pk_bf16_f32 v135, v22, v23\n" \
  "v_mfma_f32_32x32x16_bf16 v[32:47], v[148:151], v[172:175], v[32:47]\n" \
  "s_cbranch_vccnz .Lspin8_%=\n" \
  ".Lok8_%=:\n" \
  "v_exp_f32 v8, v8\n" \
  "v_exp_f32 v9, v9\n" \
  "v_exp_f32 v10, v10\n" \
  "v_exp_f32 v11, v11\n" \
  "v_cvt_pk_bf16_f32 v128, v8, v9\n" \
  "v_cvt_pk_bf16_f32 v129, v10, v11\n" \
  "v_mfma_f32_32x32x16_bf16 v[80:95], v[136:139], v[132:135], v[80:95]\n" \
  "v_exp_f32 v12, v12\n" \
  "v_exp_f32 v13, v13\n" \
  "v_exp_f32 v14, v14\n" \
  "v_exp_f32 v15, v15\n" \
  "v_cvt_pk_bf16_f32 v130, v12, v13\n" \
  "v_cvt_pk_bf16_f32 v131, v14, v15\n" \
  "v_mfma_f32_32x32x16_bf16 v[48:63], v[148:151], v[176:179], v[48:63]\n" \
  "v_exp_f32 v24, v24\n" \
  "v_exp_f32 v25, v25\n" \
  "v_exp_f32 v26, v26\n" \
  "v_exp_f32 v27, v27\n" \
  "v_cvt_pk_bf16_f32 v132, v24, v25\n" \
  "v_cvt_pk_bf16_f32 v133, v26, v27\n" \
  "v_mfma_f32_32x32x16_bf16 v[64:79], v[140:143], v[128:131], v[64:79]\n" \
  "v_exp_f32 v28, v28\n" \
  "v_exp_f32 v29, v29\n" \
  "v_exp_f32 v30, v30\n" \
  "v_exp_f32 v31, v31\n" \
  "v_cvt_pk_bf16_f32 v134, v28, v29\n" \
  "v_cvt_pk_bf16_f32 v135, v30, v31\n" \
  "v_mfma_f32_32x32x4_2b_bf16 v[0:31], v[158:159], v[180:181], 0\n" \
  "v_exp_f32 v32, v32\n" \
  "v_exp_f32 v33, v33\n" \
  "v_exp_f32 v34, v34\n" \
  "v_exp_f32 v35, v35\n" \
  "v_cvt_pk_bf16_f32 v128, v32, v33\n" \
  "v_cvt_pk_bf16_f32 v129, v34, v35\n" \
  "v_exp_f32 v36, v36\n" \
  "v_exp_f32 v37, v37\n" \
  "v_exp_f32 v38, v38\n" \
  "v_exp_f32 v39, v39\n" \
  "v_cvt_pk_bf16_f32 v130, v36, v37\n" \
  "v_cvt_pk_bf16_f32 v131, v38, v39\n" \
  "v_mfma_f32_32x32x16_bf16 v[80:95], v[140:143], v[132:135], v[80:95]\n" \
  "v_exp_f32 v48, v48\n" \
  "v_exp_f32 v49, v49\n" \
  "v_exp_f32 v50, v50\n" \
  "v_exp_f32 v51, v51\n" \
  "v_cvt_pk_bf16_f32 v132, v48, v49\n" \
  "v_cvt_pk_bf16_f32 v133, v50, v51\n" \
  "v_mfma_f32_32x32x16_bf16 v[96:111], v[136:139], v[128:131], v[96:111]\n" \
  "v_exp_f32 v52, v52\n" \
  "v_exp_f32 v53, v53\n" \
  "v_exp_f32 v54, v54\n" \
  "v_exp_f32 v55, v55\n" \
  "v_cvt_pk_bf16_f32 v134, v52, v53\n" \
  "v_cvt_pk_bf16_f32 v135, v54, v55\n" \
  "v_mfma_f32_32x32x16_bf16 v[0:15], v[152:155], v[164:167], v[0:15]\n" \
  "v_exp_f32 v40, v40\n" \
  "v_exp_f32 v41, v41\n" \
  "v_exp_f32 v42, v42\n" \
  "v_exp_f32 v43, v43\n" \
  "v_cvt_pk_bf16_f32 v128, v40, v41\n" \
  "v_cvt_pk_bf16_f32 v129, v42, v43\n" \
  "v_mfma_f32_32x32x16_bf16 v[112:127], v[136:139], v[132:135], v[112:127]\n" \
  "ds_read_b128 v[136:139], v186 offset:22336\n" \
  "ds_read_b128 v[144:147], v186 offset:23008\n" \
  "v_exp_f32 v44, v44\n" \
  "v_exp_f32 v45, v45\n" \
  "v_exp_f32 v46, v46\n" \
  "v_exp_f32 v47, v47\n" \
  "v_cvt_pk_bf16_f32 v130, v44, v45\n" \
  "v_cvt_pk_bf16_f32 v131, v46, v47\n" \
  "v_mfma_f32_32x32x16_bf16 v[16:31], v[152:155], v[168:171], v[16:31]\n" \
  "ds_read_b128 v[148:151], v184 offset:31488\n" \
  "ds_read_b128 v[160:163], v185 offset:31488\n" \
  "v_exp_f32 v56, v56\n" \
  "v_exp_f32 v57, v57\n" \
  "v_exp_f32 v58, v58\n" \
  "v_exp_f32 v59, v59\n" \
  "v_cvt_pk_bf16_f32 v132, v56, v57\n" \
  "v_cvt_pk_bf16_f32 v133, v58, v59\n" \
  "v_mfma_f32_32x32x16_bf16 v[96:111], v[140:143], v[128:131], v[96:111]\n" \
  "v_exp_f32 v60, v60\n" \
  "v_exp_f32 v61, v61\n" \
  "v_exp_f32 v62, v62\n" \
  "v_exp_f32 v63, v63\n" \
  "v_cvt_pk_bf16_f32 v134, v60, v61\n" \
  "v_cvt_pk_bf16_f32 v135, v62, v63\n" \
  "v_mfma_f32_32x32x4_2b_bf16 v[32:63], v[158:159], v[182:183], 0\n" \
  "v_exp_f32 v0, v0\n" \
  "v_exp_f32 v1, v1\n" \
  "v_exp_f32 v2, v2\n" \
  "v_exp_f32 v3, v3\n" \
  "v_cvt_pk_bf16_f32 v128, v0, v1\n" \
  "v_cvt_pk_bf16_f32 v129, v2, v3\n" \
  "v_exp_f32 v4, v4\n" \
  "v_exp_f32 v5, v5\n" \
  "v_exp_f32 v6, v6\n" \
  "v_exp_f32 v7, v7\n" \
  "v_cvt_pk_bf16_f32 v130, v4, v5\n" \
  "v_cvt_pk_bf16_f32 v131, v6, v7\n" \
  "v_mfma_f32_32x32x16_bf16 v[112:127], v[140:143], v[132:135], v[112:127]\n" \
  "v_exp_f32 v16, v16\n" \
  "v_exp_f32 v17, v17\n" \
  "v_exp_f32 v18, v18\n" \
  "v_exp_f32 v19, v19\n" \
  "v_cvt_pk_bf16_f32 v132, v16, v17\n" \
  "v_cvt_pk_bf16_f32 v133, v18, v19\n" \
  "s_waitcnt lgkmcnt(0)\n" \
  "v_mfma_f32_32x32x16_bf16 v[64:79], v[136:139], v[128:131], v[64:79]\n" \
  "ds_add_rtn_u32 v189, v187, v188 offset:8\n" \
  "v_exp_f32 v20, v20\n" \
  "v_exp_f32 v21, v21\n" \
  "v_exp_f32 v22, v22\n" \
  "v_exp_f32 v23, v23\n" \
  "v_cvt_pk_bf16_f32 v134, v20, v21\n" \
  "v_cvt_pk_bf16_f32 v135, v22, v23\n" \
  "v_mfma_f32_32x32x16_bf16 v[32:47], v[152:155], v[172:175], v[32:47]\n" \
  "v_exp_f32 v8, v8\n" \
  "v_exp_f32 v9, v9\n" \
  "v_exp_f32 v10, v10\n" \
  "v_exp_f32 v11, v11\n" \
  "v_cvt_pk_bf16_f32 v128, v8, v9\n" \
  "v_cvt_pk_bf16_f32 v129, v10, v11\n" \
  "v_mfma_f32_32x32x16_bf16 v[80:95], v[136:139], v[132:135], v[80:95]\n" \
  "v_exp_f32 v12, v12\n" \
  "v_exp_f32 v13, v13\n" \
  "v_exp_f32 v14, v14\n" \
  "v_exp_f32 v15, v15\n" \
  "v_cvt_pk_bf16_f32 v130, v12, v13\n" \
  "v_cvt_pk_bf16_f32 v131, v14, v15\n" \
  "v_mfma_f32_32x32x16_bf16 v[48:63], v[152:155], v[176:179], v[48:63]\n" \
  "v_exp_f32 v24, v24\n" \
  "v_exp_f32 v25, v25\n" \
  "v_exp_f32 v26, v26\n" \
  "v_exp_f32 v27, v27\n" \
  "v_cvt_pk_bf16_f32 v132, v24, v25\n" \
  "v_cvt_pk_bf16_f32 v133, v26, v27\n" \
  "v_mfma_f32_32x32x16_bf16 v[64:79], v[144:147], v[128:131], v[64:79]\n" \
  "v_exp_f32 v28, v28\n" \
  "v_exp_f32 v29, v29\n" \
  "v_exp_f32 v30, v30\n" \
  "v_exp_f32 v31, v31\n" \
  "v_cvt_pk_bf16_f32 v134, v28, v29\n" \
  "v_cvt_pk_bf16_f32 v135, v30, v31\n" \
  "v_mfma_f32_32x32x4_2b_bf16 v[0:31], v[160:161], v[180:181], 0\n" \
  "v_exp_f32 v32, v32\n" \
  "v_exp_f32 v33, v33\n" \
  "v_exp_f32 v34, v34\n" \
  "v_exp_f32 v35, v35\n" \
  "v_cvt_pk_bf16_f32 v128, v32, v33\n" \
  "v_cvt_pk_bf16_f32 v129, v34, v35\n" \
  "v_exp_f32 v36, v36\n" \
  "v_exp_f32 v37, v37\n" \
  "v_exp_f32 v38, v38\n" \
  "v_exp_f32 v39, v39\n" \
  "v_cvt_pk_bf16_f32 v130, v36, v37\n" \
  "v_cvt_pk_bf16_f32 v131, v38, v39\n" \
  "v_mfma_f32_32x32x16_bf16 v[80:95], v[144:147], v[132:135], v[80:95]\n" \
  "v_exp_f32 v48, v48\n" \
  "v_exp_f32 v49, v49\n" \
  "v_exp_f32 v50, v50\n" \
  "v_exp_f32 v51, v51\n" \
  "v_cvt_pk_bf16_f32 v132, v48, v49\n" \
  "v_cvt_pk_bf16_f32 v133, v50, v51\n" \
  "v_mfma_f32_32x32x16_bf16 v[96:111], v[136:139], v[128:131], v[96:111]\n" \
  "v_exp_f32 v52, v52\n" \
  "v_exp_f32 v53, v53\n" \
  "v_exp_f32 v54, v54\n" \
  "v_exp_f32 v55, v55\n" \
  "v_cvt_pk_bf16_f32 v134, v52, v53\n" \
  "v_cvt_pk_bf16_f32 v135, v54, v55\n" \
  "v_mfma_f32_32x32x16_bf16 v[0:15], v[148:151], v[164:167], v[0:15]\n" \
  "v_exp_f32 v40, v40\n" \
  "v_exp_f32 v41, v41\n" \
  "v_exp_f32 v42, v42\n" \
  "v_exp_f32 v43, v43\n" \
  "v_cvt_pk_bf16_f32 v128, v40, v41\n" \
  "v_cvt_pk_bf16_f32 v129, v42, v43\n" \
  "v_mfma_f32_32x32x16_bf16 v[112:127], v[136:139], v[132:135], v[112:127]\n" \
  "ds_read_b128 v[136:139], v186 offset:31488\n" \
  "ds_read_b128 v[140:143], v186 offset:32160\n" \
  "v_exp_f32 v44, v44\n" \
  "v_exp_f32 v45, v45\n" \
  "v_exp_f32 v46, v46\n" \
  "v_exp_f32 v47, v47\n" \
  "v_cvt_pk_bf16_f32 v130, v44, v45\n" \
  "v_cvt_pk_bf16_f32 v131, v46, v47\n" \
  "v_mfma_f32_32x32x16_bf16 v[16:31], v[148:151], v[168:171], v[16:31]\n" \
  "ds_read_b128 v[152:155], v184 offset:32512\n" \
  "v_exp_f32 v56, v56\n" \
  "v_exp_f32 v57, v57\n" \
  "v_exp_f32 v58, v58\n" \
  "v_exp_f32 v59, v59\n" \
  "v_cvt_pk_bf16_f32 v132, v56, v57\n" \
  "v_cvt_pk_bf16_f32 v133, v58, v59\n" \
  "v_mfma_f32_32x32x16_bf16 v[96:111], v[144:147], v[128:131], v[96:111]\n" \
  "v_exp_f32 v60, v60\n" \
  "v_exp_f32 v61, v61\n" \
  "v_exp_f32 v62, v62\n" \
  "v_exp_f32 v63, v63\n" \
  "v_cvt_pk_bf16_f32 v134, v60, v61\n" \
  "v_cvt_pk_bf16_f32 v135, v62, v63\n" \
  "v_mfma_f32_32x32x4_2b_bf16 v[32:63], v[160:161], v[182:183], 0\n" \
  "v_exp_f32 v0, v0\n" \
  "v_exp_f32 v1, v1\n" \
  "v_exp_f32 v2, v2\n" \
  "v_exp_f32 v3, v3\n" \
  "v_cvt_pk_bf16_f32 v128, v0, v1\n" \
  "v_cvt_pk_bf16_f32 v129, v2, v3\n" \
  "v_exp_f32 v4, v4\n" \
  "v_exp_f32 v5, v5\n" \
  "v_exp_f32 v6, v6\n" \
  "v_exp_f32 v7, v7\n" \
  "v_cvt_pk_bf16_f32 v130, v4, v5\n" \
  "v_cvt_pk_bf16_f32 v131, v6, v7\n" \
  "v_mfma_f32_32x32x16_bf16 v[112:127], v[144:147], v[132:135], v[112:127]\n" \
  "v_exp_f32 v16, v16\n" \
  "v_exp_f32 v17, v17\n" \
  "v_exp_f32 v18, v18\n" \
  "v_exp_f32 v19, v19\n" \
  "v_cvt_pk_bf16_f32 v132, v16, v17\n" \
  "v_cvt_pk_bf16_f32 v133, v18, v19\n" \
  "s_waitcnt lgkmcnt(0)\n" \
  "v_mfma_f32_32x32x16_bf16 v[64:79], v[136:139], v[128:131], v[64:79]\n" \
  "s_cmp_eq_u32 %[it], 1\n" \
  "s_cselect_b32 %[msk], 0, -1\n" \
  "v_add_u32 v190, v190, v191\n" \
  "v_and_b32 v190, %[msk], v190\n" \
  "v_cmp_gt_u32 vcc, v190, v189\n" \
  "v_exp_f32 v20, v20\n" \
  "v_exp_f32 v21, v21\n" \
  "v_exp_f32 v22, v22\n" \
  "v_exp_f32 v23, v23\n" \
  "v_cvt_pk_bf16_f32 v134, v20, v21\n" \
  "v_cvt_pk_bf16_f32 v135, v22, v23\n" \
  "v_mfma_f32_32x32x16_bf16 v[32:47], v[148:151], v[172:175], v[32:47]\n" \
  "s_cbranch_vccnz .Lspin12_%=\n" \
  ".Lok12_%=:\n" \
  "v_exp_f32 v8, v8\n" \
  "v_exp_f32 v9, v9\n" \
  "v_exp_f32 v10, v10\n" \
  "v_exp_f32 v11, v11\n" \
  "v_cvt_pk_bf16_f32 v128, v8, v9\n" \
  "v_cvt_pk_bf16_f32 v129, v10, v11\n" \
  "v_mfma_f32_32x32x16_bf16 v[80:95], v[136:139], v[132:135], v[80:95]\n" \
  "v_exp_f32 v12, v12\n" \
  "v_exp_f32 v13, v13\n" \
  "v_exp_f32 v14, v14\n" \
  "v_exp_f32 v15, v15\n" \
  "v_cvt_pk_bf16_f32 v130, v12, v13\n" \
  "v_cvt_pk_bf16_f32 v131, v14, v15\n" \
  "v_mfma_f32_32x32x16_bf16 v[48:63], v[148:151], v[176:179], v[48:63]\n" \
  "v_exp_f32 v24, v24\n" \
  "v_exp_f32 v25, v25\n" \
  "v_exp_f32 v26, v26\n" \
  "v_exp_f32 v27, v27\n" \
  "v_cvt_pk_bf16_f32 v132, v24, v25\n" \
  "v_cvt_pk_bf16_f32 v133, v26, v27\n" \
  "v_mfma_f32_32x32x16_bf16 v[64:79], v[140:143], v[128:131], v[64:79]\n" \
  "v_exp_f32 v28, v28\n" \
  "v_exp_f32 v29, v29\n" \
  "v_exp_f32 v30, v30\n" \
  "v_exp_f32 v31, v31\n" \
  "v_cvt_pk_bf16_f32 v134, v28, v29\n" \
  "v_cvt_pk_bf16_f32 v135, v30, v31\n" \
  "v_mfma_f32_32x32x4_2b_bf16 v[0:31], v[162:163], v[180:181], 0\n" \
  "v_exp_f32 v32, v32\n" \
  "v_exp_f32 v33, v33\n" \
  "v_exp_f32 v34, v34\n" \
  "v_exp_f32 v35, v35\n" \
  "v_cvt_pk_bf16_f32 v128, v32, v33\n" \
  "v_cvt_pk_bf16_f32 v129, v34, v35\n" \
  "v_exp_f32 v36, v36\n" \
  "v_exp_f32 v37, v37\n" \
  "v_exp_f32 v38, v38\n" \
  "v_exp_f32 v39, v39\n" \
  "v_cvt_pk_bf16_f32 v130, v36, v37\n" \
  "v_cvt_pk_bf16_f32 v131, v38, v39\n" \
  "v_mfma_f32_32x32x16_bf16 v[80:95], v[140:143], v[132:135], v[80:95]\n" \
  "v_exp_f32 v48, v48\n" \
  "v_exp_f32 v49, v49\n" \
  "v_exp_f32 v50, v50\n" \
  "v_exp_f32 v51, v51\n" \
  "v_cvt_pk_bf16_f32 v132, v48, v49\n" \
  "v_cvt_pk_bf16_f32 v133, v50, v51\n" \
  "v_mfma_f32_32x32x16_bf16 v[96:111], v[136:139], v[128:131], v[96:111]\n" \
  "v_exp_f32 v52, v52\n" \
  "v_exp_f32 v53, v53\n" \
  "v_exp_f32 v54, v54\n" \
  "v_exp_f32 v55, v55\n" \
  "v_cvt_pk_bf16_f32 v134, v52, v53\n" \
  "v_cvt_pk_bf16_f32 v135, v54, v55\n" \
  "v_mfma_f32_32x32x16_bf16 v[0:15], v[152:155], v[164:167], v[0:15]\n" \
  "v_exp_f32 v40, v40\n" \
  "v_exp_f32 v41, v41\n" \
  "v_exp_f32 v42, v42\n" \
  "v_exp_f32 v43, v43\n" \
  "v_cvt_pk_bf16_f32 v128, v40, v41\n" \
  "v_cvt_pk_bf16_f32 v129, v42, v43\n" \
  "v_mfma_f32_32x32x16_bf16 v[112:127], v[136:139], v[132:135], v[112:127]\n" \
  "ds_read_b128 v[136:139], v186 offset:32832\n" \
  "ds_read_b128 v[144:147], v186 offset:33504\n" \
  "v_exp_f32 v44, v44\n" \
  "v_exp_f32 v45, v45\n" \
  "v_exp_f32 v46, v46\n" \
  "v_exp_f32 v47, v47\n" \
  "v_cvt_pk_bf16_f32 v130, v44, v45\n" \
  "v_cvt_pk_bf16_f32 v131, v46, v47\n" \
  "v_mfma_f32_32x32x16_bf16 v[16:31], v[152:155], v[168:171], v[16:31]\n" \
  "ds_read_b128 v[148:151], v184 offset:0\n" \
  "ds_read_b128 v[156:159], v185 offset:0\n" \
  "v_exp_f32 v56, v56\n" \
  "v_exp_f32 v57, v57\n" \
  "v_exp_f32 v58, v58\n" \
  "v_exp_f32 v59, v59\n" \
  "v_cvt_pk_bf16_f32 v132, v56, v57\n" \
  "v_cvt_pk_bf16_f32 v133, v58, v59\n" \
  "v_mfma_f32_32x32x16_bf16 v[96:111], v[140:143], v[128:131], v[96:111]\n" \
  "v_exp_f32 v60, v60\n" \
  "v_exp_f32 v61, v61\n" \
  "v_exp_f32 v62, v62\n" \
  "v_exp_f32 v63, v63\n" \
  "v_cvt_pk_bf16_f32 v134, v60, v61\n" \
  "v_cvt_pk_bf16_f32 v135, v62, v63\n" \
  "v_mfma_f32_32x32x4_2b_bf16 v[32:63], v[162:163], v[182:183], 0\n" \
  "v_exp_f32 v0, v0\n" \
  "v_exp_f32 v1, v1\n" \
  "v_exp_f32 v2, v2\n" \
  "v_exp_f32 v3, v3\n" \
  "v_cvt_pk_bf16_f32 v128, v0, v1\n" \
  "v_cvt_pk_bf16_f32 v129, v2, v3\n" \
  "v_exp_f32 v4, v4\n" \
  "v_exp_f32 v5, v5\n" \
  "v_exp_f32 v6, v6\n" \
  "v_exp_f32 v7, v7\n" \
  "v_cvt_pk_bf16_f32 v130, v4, v5\n" \
  "v_cvt_pk_bf16_f32 v131, v6, v7\n" \
  "v_mfma_f32_32x32x16_bf16 v[112:127], v[140:143], v[132:135], v[112:127]\n" \
  "v_exp_f32 v16, v16\n" \
  "v_exp_f32 v17, v17\n" \
  "v_exp_f32 v18, v18\n" \
  "v_exp_f32 v19, v19\n" \
  "v_cvt_pk_bf16_f32 v132, v16, v17\n" \
  "v_cvt_pk_bf16_f32 v133, v18, v19\n" \
  "s_waitcnt lgkmcnt(0)\n" \
  "v_mfma_f32_32x32x16_bf16 v[64:79], v[136:139], v[128:131], v[64:79]\n" \
  "ds_add_rtn_u32 v189, v187, v188 offset:12\n" \
  "v_exp_f32 v20, v20\n" \
  "v_exp_f32 v21, v21\n" \
  "v_exp_f32 v22, v22\n" \
  "v_exp_f32 v23, v23\n" \
  "v_cvt_pk_bf16_f32 v134, v20, v21\n" \
  "v_cvt_pk_bf16_f32 v135, v22, v23\n" \
  "v_mfma_f32_32x32x16_bf16 v[32:47], v[152:155], v[172:175], v[32:47]\n" \
  "v_exp_f32 v8, v8\n" \
  "v_exp_f32 v9, v9\n" \
  "v_exp_f32 v10, v10\n" \
  "v_exp_f32 v11, v11\n" \
  "v_cvt_pk_bf16_f32 v128, v8, v9\n" \
  "v_cvt_pk_bf16_f32 v129, v10, v11\n" \
  "v_mfma_f32_32x32x16_bf16 v[80:95], v[136:139], v[132:135], v[80:95]\n" \
  "v_exp_f32 v12, v12\n" \
  "v_exp_f32 v13, v13\n" \
  "v_exp_f32 v14, v14\n" \
  "v_exp_f32 v15, v15\n" \
  "v_cvt_pk_bf16_f32 v130, v12, v13\n" \
  "v_cvt_pk_bf16_f32 v131, v14, v15\n" \
  "v_mfma_f32_32x32x16_bf16 v[48:63], v[152:155], v[176:179], v[48:63]\n" \
  "v_exp_f32 v24, v24\n" \
  "v_exp_f32 v25, v25\n" \
  "v_exp_f32 v26, v26\n" \
  "v_exp_f32 v27, v27\n" \
  "v_cvt_pk_bf16_f32 v132, v24, v25\n" \
  "v_cvt_pk_bf16_f32 v133, v26, v27\n" \
  "v_mfma_f32_32x32x16_bf16 v[64:79], v[144:147], v[128:131], v[64:79]\n" \
  "v_exp_f32 v28, v28\n" \
  "v_exp_f32 v29, v29\n" \
  "v_exp_f32 v30, v30\n" \
  "v_exp_f32 v31, v31\n" \
  "v_cvt_pk_bf16_f32 v134, v28, v29\n" \
  "v_cvt_pk_bf16_f32 v135, v30, v31\n" \
  "v_mfma_f32_32x32x4_2b_bf16 v[0:31], v[156:157], v[180:181], 0\n" \
  "v_exp_f32 v32, v32\n" \
  "v_exp_f32 v33, v33\n" \
  "v_exp_f32 v34, v34\n" \
  "v_exp_f32 v35, v35\n" \
  "v_cvt_pk_bf16_f32 v128, v32, v33\n" \
  "v_cvt_pk_bf16_f32 v129, v34, v35\n" \
  "v_exp_f32 v36, v36\n" \
  "v_exp_f32 v37, v37\n" \
  "v_exp_f32 v38, v38\n" \
  "v_exp_f32 v39, v39\n" \
  "v_cvt_pk_bf16_f32 v130, v36, v37\n" \
  "v_cvt_pk_bf16_f32 v131, v38, v39\n" \
  "v_mfma_f32_32x32x16_bf16 v[80:95], v[144:147], v[132:135], v[80:95]\n" \
  "v_exp_f32 v48, v48\n" \
  "v_exp_f32 v49, v49\n" \
  "v_exp_f32 v50, v50\n" \
  "v_exp_f32 v51, v51\n" \
  "v_cvt_pk_bf16_f32 v132, v48, v49\n" \
  "v_cvt_pk_bf16_f32 v133, v50, v51\n" \
  "v_mfma_f32_32x32x16_bf16 v[96:111], v[136:139], v[128:131], v[96:111]\n" \
  "v_exp_f32 v52, v52\n" \
  "v_exp_f32 v53, v53\n" \
  "v_exp_f32 v54, v54\n" \
  "v_exp_f32 v55, v55\n" \
  "v_cvt_pk_bf16_f32 v134, v52, v53\n" \
  "v_cvt_pk_bf16_f32 v135, v54, v55\n" \
  "v_mfma_f32_32x32x16_bf16 v[0:15], v[148:151], v[164:167], v[0:15]\n" \
  "v_exp_f32 v40, v40\n" \
  "v_exp_f32 v41, v41\n" \
  "v_exp_f32 v42, v42\n" \
  "v_exp_f32 v43, v43\n" \
  "v_cvt_pk_bf16_f32 v128, v40, v41\n" \
  "v_cvt_pk_bf16_f32 v129, v42, v43\n" \
  "v_mfma_f32_32x32x16_bf16 v[112:127], v[136:139], v[132:135], v[112:127]\n" \
  "ds_read_b128 v[136:139], v186 offset:0\n" \
  "ds_read_b128 v[140:143], v186 offset:672\n" \
  "v_exp_f32 v44, v44\n" \
  "v_exp_f32 v45, v45\n" \
  "v_exp_f32 v46, v46\n" \
  "v_exp_f32 v47, v47\n" \
  "v_cvt_pk_bf16_f32 v130, v44, v45\n" \
  "v_cvt_pk_bf16_f32 v131, v46, v47\n" \
  "v_mfma_f32_32x32x16_bf16 v[16:31], v[148:151], v[168:171], v[16:31]\n" \
  "ds_read_b128 v[152:155], v184 offset:1024\n" \
  "v_exp_f32 v56, v56\n" \
  "v_exp_f32 v57, v57\n" \
  "v_exp_f32 v58, v58\n" \
  "v_exp_f32 v59, v59\n" \
  "v_cvt_pk_bf16_f32 v132, v56, v57\n" \
  "v_cvt_pk_bf16_f32 v133, v58, v59\n" \
  "v_mfma_f32_32x32x16_bf16 v[96:111], v[144:147], v[128:131], v[96:111]\n" \
  "v_exp_f32 v60, v60\n" \
  "v_exp_f32 v61, v61\n" \
  "v_exp_f32 v62, v62\n" \
  "v_exp_f32 v63, v63\n" \
  "v_cvt_pk_bf16_f32 v134, v60, v61\n" \
  "v_cvt_pk_bf16_f32 v135, v62, v63\n" \
  "s_sub_u32 %[it], %[it], 1\n" \
  "s_cmp_lg_u32 %[it], 0\n" \
  "s_cbranch_scc1 .Lloop_%=\n" \
  "ds_read_b64 v[128:129], v187\n" \
  "v_mfma_f32_32x32x16_bf16 v[112:127], v[144:147], v[132:135], v[112:127]\n" \
  "s_branch .Lend_%=\n" \
  ".Lspin0_%=:\n" \
  "s_sleep 1\n" \
  "ds_read_b32 v189, v187 offset:12\n" \
  "s_sub_u32 %[spin], %[spin], 1\n" \
  "s_cbranch_scc1 .Lok0_%=\n" \
  "s_waitcnt lgkmcnt(0)\n" \
  "v_cmp_gt_u32 vcc, v190, v189\n" \
  "s_cbranch_vccnz .Lspin0_%=\n" \
  "s_branch .Lok0_%=\n" \
  ".Lspin4_%=:\n" \
  "s_sleep 1\n" \
  "ds_read_b32 v189, v187 offset:0\n" \
  "s_sub_u32 %[spin], %[spin], 1\n" \
  "s_cbranch_scc1 .Lok4_%=\n" \
  "s_waitcnt lgkmcnt(0)\n" \
  "v_cmp_gt_u32 vcc, v190, v189\n" \
  "s_cbranch_vccnz .Lspin4_%=\n" \
  "s_branch .Lok4_%=\n" \
  ".Lspin8_%=:\n" \
  "s_sleep 1\n" \
  "ds_read_b32 v189, v187 offset:4\n" \
  "s_sub_u32 %[spin], %[spin], 1\n" \
  "s_cbranch_scc1 .Lok8_%=\n" \
  "s_waitcnt lgkmcnt(0)\n" \
  "v_cmp_gt_u32 vcc, v190, v189\n" \
  "s_cbranch_vccnz .Lspin8_%=\n" \
  "s_branch .Lok8_%=\n" \
  ".Lspin12_%=:\n" \
  "s_sleep 1\n" \
  "ds_read_b32 v189, v187 offset:8\n" \
  "s_sub_u32 %[spin], %[spin], 1\n" \
  "s_cbranch_scc1 .Lok12_%=\n" \
  "s_waitcnt lgkmcnt(0)\n" \
  "v_cmp_gt_u32 vcc, v190, v189\n" \
  "s_cbranch_vccnz .Lspin12_%=\n" \
  "s_branch .Lok12_%=\n" \
  ".Lend_%=:\n" \
  "s_waitcnt lgkmcnt(0)\n" \
  "s_nop 9\n" \
  ""
#define OPROJ_PRE \
  "ds_read_b64_tr_b16 v[60:61], v160 offset:0\n" \
  "ds_read_b64_tr_b16 v[62:63], v160 offset:2560\n" \
  "ds_read_b64_tr_b16 v[64:65], v160 offset:32\n" \
  "ds_read_b64_tr_b16 v[66:67], v160 offset:2592\n" \
  "ds_read_b64_tr_b16 v[68:69], v160 offset:64\n" \
  "ds_read_b64_tr_b16 v[70:71], v160 offset:2624\n" \
  "ds_read_b64_tr_b16 v[72:73], v160 offset:96\n" \
  "ds_read_b64_tr_b16 v[74:75], v160 offset:2656\n" \
  "ds_read_b64_tr_b16 v[76:77], v160 offset:128\n" \
  "ds_read_b64_tr_b16 v[78:79], v160 offset:2688\n" \
  "ds_read_b64_tr_b16 v[80:81], v160 offset:5120\n" \
  "ds_read_b64_tr_b16 v[82:83], v160 offset:7680\n" \
  "ds_read_b64_tr_b16 v[84:85], v160 offset:5152\n" \
  "ds_read_b64_tr_b16 v[86:87], v160 offset:7712\n" \
  "s_waitcnt lgkmcnt(12)\n" \
  "ds_read_b64_tr_b16 v[88:89], v160 offset:5184\n" \
  "ds_read_b64_tr_b16 v[90:91], v160 offset:7744\n" \
  "s_waitcnt lgkmcnt(12)\n" \
  "ds_read_b64_tr_b16 v[92:93], v160 offset:5216\n" \
  "ds_read_b64_tr_b16 v[94:95], v160 offset:7776\n" \
  "s_waitcnt lgkmcnt(12)\n" \
  "ds_read_b64_tr_b16 v[96:97], v160 offset:5248\n" \
  "ds_read_b64_tr_b16 v[98:99], v160 offset:7808\n" \
  "s_waitcnt lgkmcnt(12)\n" \
  "ds_read_b64_tr_b16 v[100:101], v160 offset:10240\n" \
  "ds_read_b64_tr_b16 v[102:103], v160 offset:12800\n" \
  "s_waitcnt lgkmcnt(12)\n" \
  "ds_read_b64_tr_b16 v[104:105], v160 offset:10272\n" \
  "ds_read_b64_tr_b16 v[106:107], v160 offset:12832\n" \
  "s_waitcnt lgkmcnt(12)\n" \
  "ds_read_b64_tr_b16 v[108:109], v160 offset:10304\n" \
  "ds_read_b64_tr_b16 v[110:111], v160 offset:12864\n" \
  "s_waitcnt lgkmcnt(12)\n" \
  "ds_read_b64_tr_b16 v[112:113], v160 offset:10336\n" \
  "ds_read_b64_tr_b16 v[114:115], v160 offset:12896\n" \
  "s_waitcnt lgkmcnt(12)\n" \
  "ds_read_b64_tr_b16 v[116:117], v160 offset:10368\n" \
  "ds_read_b64_tr_b16 v[118:119], v160 offset:12928\n" \
  "s_waitcnt lgkmcnt(12)\n" \
  "ds_read_b64_tr_b16 v[120:121], v160 offset:15360\n" \
  "ds_read_b64_tr_b16 v[122:123], v160 offset:17920\n" \
  "s_waitcnt lgkmcnt(12)\n" \
  "ds_read_b64_tr_b16 v[124:125], v160 offset:15392\n" \
  "ds_read_b64_tr_b16 v[126:127], v160 offset:17952\n" \
  "s_waitcnt lgkmcnt(12)\n" \
  "ds_read_b64_tr_b16 v[128:129], v160 offset:15424\n" \
  "ds_read_b64_tr_b16 v[130:131], v160 offset:17984\n" \
  "s_waitcnt lgkmcnt(12)\n" \
  "ds_read_b64_tr_b16 v[132:133], v160 offset:15456\n" \
  "ds_read_b64_tr_b16 v[134:135], v160 offset:18016\n" \
  "s_waitcnt lgkmcnt(12)\n" \
  "ds_read_b64_tr_b16 v[136:137], v160 offset:15488\n" \
  "ds_read_b64_tr_b16 v[138:139], v160 offset:18048\n" \
  "s_waitcnt lgkmcnt(12)\n" \
  "ds_read_b64_tr_b16 v[140:141], v160 offset:20480\n" \
  "ds_read_b64_tr_b16 v[142:143], v160 offset:23040\n" \
  "s_waitcnt lgkmcnt(12)\n" \
  "ds_read_b64_tr_b16 v[144:145], v160 offset:20512\n" \
  "ds_read_b64_tr_b16 v[146:147], v160 offset:23072\n" \
  "s_waitcnt lgkmcnt(12)\n" \
  "ds_read_b64_tr_b16 v[148:149], v160 offset:20544\n" \
  "ds_read_b64_tr_b16 v[150:151], v160 offset:23104\n" \
  "s_waitcnt lgkmcnt(12)\n" \
  "ds_read_b64_tr_b16 v[152:153], v160 offset:20576\n" \
  "ds_read_b64_tr_b16 v[154:155], v160 offset:23136\n" \
  "s_waitcnt lgkmcnt(12)\n" \
  "ds_read_b64_tr_b16 v[156:157], v160 offset:20608\n" \
  "ds_read_b64_tr_b16 v[158:159], v160 offset:23168\n" \
  "s_waitcnt lgkmcnt(0)\n" \
  ""
#define OPROJ_FIRST \
  "s_nop 1\n" \
  "v_mfma_f32_16x16x32_bf16 v[20:23], v[60:63], v[0:3], 0\n" \
  "v_mfma_f32_16x16x32_bf16 v[24:27], v[64:67], v[0:3], 0\n" \
  "ds_read_b64_tr_b16 v[60:61], v160 offset:25600\n" \
  "ds_read_b64_tr_b16 v[62:63], v160 offset:28160\n" \
  "v_mfma_f32_16x16x32_bf16 v[28:31], v[68:71], v[0:3], 0\n" \
  "ds_read_b64_tr_b16 v[64:65], v160 offset:25632\n" \
  "ds_read_b64_tr_b16 v[66:67], v160 offset:28192\n" \
  "v_mfma_f32_16x16x32_bf16 v[32:35], v[72:75], v[0:3], 0\n" \
  "ds_read_b64_tr_b16 v[68:69], v160 offset:25664\n" \
  "ds_read_b64_tr_b16 v[70:71], v160 offset:28224\n" \
  "v_mfma_f32_16x16x32_bf16 v[36:39], v[76:79], v[0:3], 0\n" \
  "ds_read_b64_tr_b16 v[72:73], v160 offset:25696\n" \
  "ds_read_b64_tr_b16 v[74:75], v160 offset:28256\n" \
  "v_mfma_f32_16x16x32_bf16 v[20:23], v[80:83], v[4:7], v[20:23]\n" \
  "ds_read_b64_tr_b16 v[76:77], v160 offset:25728\n" \
  "ds_read_b64_tr_b16 v[78:79], v160 offset:28288\n" \
  "v_mfma_f32_16x16x32_bf16 v[24:27], v[84:87], v[4:7], v[24:27]\n" \
  "ds_read_b64_tr_b16 v[80:81], v160 offset:30720\n" \
  "ds_read_b64_tr_b16 v[82:83], v160 offset:33280\n" \
  "v_mfma_f32_16x16x32_bf16 v[28:31], v[88:91], v[4:7], v[28:31]\n" \
  "ds_read_b64_tr_b16 v[84:85], v160 offset:30752\n" \
  "ds_read_b64_tr_b16 v[86:87], v160 offset:33312\n" \
  "v_mfma_f32_16x16x32_bf16 v[32:35], v[92:95], v[4:7], v[32:35]\n" \
  "s_waitcnt lgkmcnt(12)\n" \
  "ds_read_b64_tr_b16 v[88:89], v160 offset:30784\n" \
  "ds_read_b64_tr_b16 v[90:91], v160 offset:33344\n" \
  "v_mfma_f32_16x16x32_bf16 v[36:39], v[96:99], v[4:7], v[36:39]\n" \
  "s_waitcnt lgkmcnt(12)\n" \
  "ds_read_b64_tr_b16 v[92:93], v160 offset:30816\n" \
  "ds_read_b64_tr_b16 v[94:95], v160 offset:33376\n" \
  "v_mfma_f32_16x16x32_bf16 v[20:23], v[100:103], v[8:11], v[20:23]\n" \
  "s_waitcnt lgkmcnt(12)\n" \
  "ds_read_b64_tr_b16 v[96:97], v160 offset:30848\n" \
  "ds_read_b64_tr_b16 v[98:99], v160 offset:33408\n" \
  "v_mfma_f32_16x16x32_bf16 v[24:27], v[104:107], v[8:11], v[24:27]\n" \
  "s_waitcnt lgkmcnt(12)\n" \
  "ds_read_b64_tr_b16 v[100:101], v160 offset:35840\n" \
  "ds_read_b64_tr_b16 v[102:103], v160 offset:38400\n" \
  "v_mfma_f32_16x16x32_bf16 v[28:31], v[108:111], v[8:11], v[28:31]\n" \
  "s_waitcnt lgkmcnt(12)\n" \
  "ds_read_b64_tr_b16 v[104:105], v160 offset:35872\n" \
  "ds_read_b64_tr_b16 v[106:107], v160 offset:38432\n" \
  "v_mfma_f32_16x16x32_bf16 v[32:35], v[112:115], v[8:11], v[32:35]\n" \
  "s_waitcnt lgkmcnt(12)\n" \
  "ds_read_b64_tr_b16 v[108:109], v160 offset:35904\n" \
  "ds_read_b64_tr_b16 v[110:111], v160 offset:38464\n" \
  "v_mfma_f32_16x16x32_bf16 v[36:39], v[116:119], v[8:11], v[36:39]\n" \
  "s_waitcnt lgkmcnt(12)\n" \
  "ds_read_b64_tr_b16 v[112:113], v160 offset:35936\n" \
  "ds_read_b64_tr_b16 v[114:115], v160 offset:38496\n" \
  "v_mfma_f32_16x16x32_bf16 v[20:23], v[120:123], v[12:15], v[20:23]\n" \
  "s_waitcnt lgkmcnt(12)\n" \
  "ds_read_b64_tr_b16 v[116:117], v160 offset:35968\n" \
  "ds_read_b64_tr_b16 v[118:119], v160 offset:38528\n" \
  "v_mfma_f32_16x16x32_bf16 v[24:27], v[124:127], v[12:15], v[24:27]\n" \
  "s_waitcnt lgkmcnt(12)\n" \
  "ds_read_b64_tr_b16 v[120:121], v160 offset:40960\n" \
  "ds_read_b64_tr_b16 v[122:123], v160 offset:43520\n" \
  "v_mfma_f32_16x16x32_bf16 v[28:31], v[128:131], v[12:15], v[28:31]\n" \
  "s_waitcnt lgkmcnt(12)\n" \
  "ds_read_b64_tr_b16 v[124:125], v160 offset:40992\n" \
  "ds_read_b64_tr_b16 v[126:127], v160 offset:43552\n" \
  "v_mfma_f32_16x16x32_bf16 v[32:35], v[132:135], v[12:15], v[32:35]\n" \
  "s_waitcnt lgkmcnt(12)\n" \
  "ds_read_b64_tr_b16 v[128:129], v160 offset:41024\n" \
  "ds_read_b64_tr_b16 v[130:131], v160 offset:43584\n" \
  "v_mfma_f32_16x16x32_bf16 v[36:39], v[136:139], v[12:15], v[36:39]\n" \
  "s_waitcnt lgkmcnt(12)\n" \
  "ds_read_b64_tr_b16 v[132:133], v160 offset:41056\n" \
  "ds_read_b64_tr_b16 v[134:135], v160 offset:43616\n" \
  "v_mfma_f32_16x16x32_bf16 v[20:23], v[140:143], v[16:19], v[20:23]\n" \
  "s_waitcnt lgkmcnt(12)\n" \
  "ds_read_b64_tr_b16 v[136:137], v160 offset:41088\n" \
  "ds_read_b64_tr_b16 v[138:139], v160 offset:43648\n" \
  "v_mfma_f32_16x16x32_bf16 v[24:27], v[144:147], v[16:19], v[24:27]\n" \
  "s_waitcnt lgkmcnt(12)\n" \
  "ds_read_b64_tr_b16 v[140:141], v160 offset:46080\n" \
  "ds_read_b64_tr_b16 v[142:143], v160 offset:48640\n" \
  "v_mfma_f32_16x16x32_bf16 v[28:31], v[148:151], v[16:19], v[28:31]\n" \
  "s_waitcnt lgkmcnt(12)\n" \
  "ds_read_b64_tr_b16 v[144:145], v160 offset:46112\n" \
  "ds_read_b64_tr_b16 v[146:147], v160 offset:48672\n" \
  "v_mfma_f32_16x16x32_bf16 v[32:35], v[152:155], v[16:19], v[32:35]\n" \
  "s_waitcnt lgkmcnt(12)\n" \
  "ds_read_b64_tr_b16 v[148:149], v160 offset:46144\n" \
  "ds_read_b64_tr_b16 v[150:151], v160 offset:48704\n" \
  "v_mfma_f32_16x16x32_bf16 v[36:39], v[156:159], v[16:19], v[36:39]\n" \
  "s_waitcnt lgkmcnt(12)\n" \
  "ds_read_b64_tr_b16 v[152:153], v160 offset:46176\n" \
  "ds_read_b64_tr_b16 v[154:155], v160 offset:48736\n" \
  "s_waitcnt lgkmcnt(12)\n" \
  "ds_read_b64_tr_b16 v[156:157], v160 offset:46208\n" \
  "ds_read_b64_tr_b16 v[158:159], v160 offset:48768\n" \
  ""
#define OPROJ_SECOND \
  "s_nop 1\n" \
  "v_mfma_f32_16x16x32_bf16 v[20:23], v[60:63], v[40:43], v[20:23]\n" \
  "v_mfma_f32_16x16x32_bf16 v[24:27], v[64:67], v[40:43], v[24:27]\n" \
  "v_mfma_f32_16x16x32_bf16 v[28:31], v[68:71], v[40:43], v[28:31]\n" \
  "v_mfma_f32_16x16x32_bf16 v[32:35], v[72:75], v[40:43], v[32:35]\n" \
  "v_mfma_f32_16x16x32_bf16 v[36:39], v[76:79], v[40:43], v[36:39]\n" \
  "v_mfma_f32_16x16x32_bf16 v[20:23], v[80:83], v[44:47], v[20:23]\n" \
  "v_mfma_f32_16x16x32_bf16 v[24:27], v[84:87], v[44:47], v[24:27]\n" \
  "v_mfma_f32_16x16x32_bf16 v[28:31], v[88:91], v[44:47], v[28:31]\n" \
  "v_mfma_f32_16x16x32_bf16 v[32:35], v[92:95], v[44:47], v[32:35]\n" \
  "v_mfma_f32_16x16x32_bf16 v[36:39], v[96:99], v[44:47], v[36:39]\n" \
  "v_mfma_f32_16x16x32_bf16 v[20:23], v[100:103], v[48:51], v[20:23]\n" \
  "v_mfma_f32_16x16x32_bf16 v[24:27], v[104:107], v[48:51], v[24:27]\n" \
  "v_mfma_f32_16x16x32_bf16 v[28:31], v[108:111], v[48:51], v[28:31]\n" \
  "v_mfma_f32_16x16x32_bf16 v[32:35], v[112:115], v[48:51], v[32:35]\n" \
  "v_mfma_f32_16x16x32_bf16 v[36:39], v[116:119], v[48:51], v[36:39]\n" \
  "v_mfma_f32_16x16x32_bf16 v[20:23], v[120:123], v[52:55], v[20:23]\n" \
  "v_mfma_f32_16x16x32_bf16 v[24:27], v[124:127], v[52:55], v[24:27]\n" \
  "v_mfma_f32_16x16x32_bf16 v[28:31], v[128:131], v[52:55], v[28:31]\n" \
  "s_waitcnt lgkmcnt(12)\n" \
  "v_mfma_f32_16x16x32_bf16 v[32:35], v[132:135], v[52:55], v[32:35]\n" \
  "s_waitcnt lgkmcnt(10)\n" \
  "v_mfma_f32_16x16x32_bf16 v[36:39], v[136:139], v[52:55], v[36:39]\n" \
  "s_waitcnt lgkmcnt(8)\n" \
  "v_mfma_f32_16x16x32_bf16 v[20:23], v[140:143], v[56:59], v[20:23]\n" \
  "s_waitcnt lgkmcnt(6)\n" \
  "v_mfma_f32_16x16x32_bf16 v[24:27], v[144:147], v[56:59], v[24:27]\n" \
  "s_waitcnt lgkmcnt(4)\n" \
  "v_mfma_f32_16x16x32_bf16 v[28:31], v[148:151], v[56:59], v[28:31]\n" \
  "s_waitcnt lgkmcnt(2)\n" \
  "v_mfma_f32_16x16x32_bf16 v[32:35], v[152:155], v[56:59], v[32:35]\n" \
  "s_waitcnt lgkmcnt(0)\n" \
  "v_mfma_f32_16x16x32_bf16 v[36:39], v[156:159], v[56:59], v[36:39]\n" \
  "s_nop 7\n" \
  ""
#define GEMM_ASM_QK \
  "v_mov_b32 v0, 0\n" \
  "v_mov_b32 v1, 0\n" \
  "v_mov_b32 v2, 0\n" \
  "v_mov_b32 v3, 0\n" \
  "v_mov_b32 v4, 0\n" \
  "v_mov_b32 v5, 0\n" \
  "v_mov_b32 v6, 0\n" \
  "v_mov_b32 v7, 0\n" \
  "v_mov_b32 v8, 0\n" \
  "v_mov_b32 v9, 0\n" \
  "v_mov_b32 v10, 0\n" \
  "v_mov_b32 v11, 0\n" \
  "v_mov_b32 v12, 0\n" \
  "v_mov_b32 v13, 0\n" \
  "v_mov_b32 v14, 0\n" \
  "v_mov_b32 v15, 0\n" \
  "v_mov_b32 v16, 0\n" \
  "v_mov_b32 v17, 0\n" \
  "v_mov_b32 v18, 0\n" \
  "v_mov_b32 v19, 0\n" \
  "v_mov_b32 v20, 0\n" \
  "v_mov_b32 v21, 0\n" \
  "v_mov_b32 v22, 0\n" \
  "v_mov_b32 v23, 0\n" \
  "v_mov_b32 v24, 0\n" \
  "v_mov_b32 v25, 0\n" \
  "v_mov_b32 v26, 0\n" \
  "v_mov_b32 v27, 0\n" \
  "v_mov_b32 v28, 0\n" \
  "v_mov_b32 v29, 0\n" \
  "v_mov_b32 v30, 0\n" \
  "v_mov_b32 v31, 0\n" \
  "v_mov_b32 v32, 0\n" \
  "v_mov_b32 v33, 0\n" \
  "v_mov_b32 v34, 0\n" \
  "v_mov_b32 v35, 0\n" \
  "v_mov_b32 v36, 0\n" \
  "v_mov_b32 v37, 0\n" \
  "v_mov_b32 v38, 0\n" \
  "v_mov_b32 v39, 0\n" \
  "v_mov_b32 v40, 0\n" \
  "v_mov_b32 v41, 0\n" \
  "v_mov_b32 v42, 0\n" \
  "v_mov_b32 v43, 0\n" \
  "v_mov_b32 v44, 0\n" \
  "v_mov_b32 v45, 0\n" \
  "v_mov_b32 v46, 0\n" \
  "v_mov_b32 v47, 0\n" \
  "v_mov_b32 v48, 0\n" \
  "v_mov_b32 v49, 0\n" \
  "v_mov_b32 v50, 0\n" \
  "v_mov_b32 v51, 0\n" \
  "v_mov_b32 v52, 0\n" \
  "v_mov_b32 v53, 0\n" \
  "v_mov_b32 v54, 0\n" \
  "v_mov_b32 v55, 0\n" \
  "v_mov_b32 v56, 0\n" \
  "v_mov_b32 v57, 0\n" \
  "v_mov_b32 v58, 0\n" \
  "v_mov_b32 v59, 0\n" \
  "v_mov_b32 v60, 0\n" \
  "v_mov_b32 v61, 0\n" \
  "v_mov_b32 v62, 0\n" \
  "v_mov_b32 v63, 0\n" \
  "v_mov_b32 v138, 0\n" \
  "v_mov_b32 v139, 0\n" \
  "v_mov_b32 v142, 0\n" \
  "v_mov_b32 v143, 0\n" \
  "v_mov_b32 v146, 0\n" \
  "v_mov_b32 v147, 0\n" \
  "v_mov_b32 v150, 0\n" \
  "v_mov_b32 v151, 0\n" \
  "s_barrier\n" \
  "ds_read_b64_tr_b16 v[72:73], v132 offset:16384\n" \
  "ds_read_b64_tr_b16 v[74:75], v132 offset:17408\n" \
  "ds_read_b128 v[64:67], v128\n" \
  "ds_read_b128 v[68:71], v128 offset:4096\n" \
  "ds_read_b64_tr_b16 v[76:77], v133 offset:16384\n" \
  "ds_read_b64_tr_b16 v[78:79], v133 offset:17408\n" \
  "ds_read_b64 v[136:137], v152\n" \
  "ds_read_b64 v[140:141], v152 offset:256\n" \
  "ds_read_b64 v[144:145], v153\n" \
  "ds_read_b64 v[148:149], v153 offset:256\n" \
  "ds_read_b64_tr_b16 v[88:89], v132 offset:20480\n" \
  "ds_read_b64_tr_b16 v[90:91], v132 offset:21504\n" \
  "ds_read_b128 v[80:83], v129\n" \
  "ds_read_b128 v[84:87], v129 offset:4096\n" \
  "ds_read_b64_tr_b16 v[92:93], v133 offset:20480\n" \
  "ds_read_b64_tr_b16 v[94:95], v133 offset:21504\n" \
  ".Lg_%=:\n" \
  "s_waitcnt lgkmcnt(10)\n" \
  "v_mfma_f32_32x32x16_bf16 v[0:15], v[72:75], v[64:67], v[0:15]\n" \
  "ds_read_b64_tr_b16 v[104:105], v132 offset:24576\n" \
  "ds_read_b64_tr_b16 v[106:107], v132 offset:25600\n" \
  "ds_read_b128 v[96:99], v130\n" \
  "v_mfma_f32_32x32x16_bf16 v[32:47], v[72:75], v[68:71], v[32:47]\n" \
  "ds_read_b128 v[100:103], v130 offset:4096\n" \
  "ds_read_b64_tr_b16 v[108:109], v133 offset:24576\n" \
  "ds_read_b64_tr_b16 v[110:111], v133 offset:25600\n" \
  "v_mfma_f32_32x32x16_bf16 v[48:63], v[76:79], v[68:71], v[48:63]\n" \
  "v_mfma_f32_32x32x16_bf16 v[16:31], v[76:79], v[64:67], v[16:31]\n" \
  "s_waitcnt lgkmcnt(6)\n" \
  "v_mfma_f32_32x32x16_bf16 v[0:15], v[88:91], v[80:83], v[0:15]\n" \
  "ds_read_b64_tr_b16 v[120:121], v132 offset:28672\n" \
  "ds_read_b64_tr_b16 v[122:123], v132 offset:29696\n" \
  "ds_read_b128 v[112:115], v131\n" \
  "v_mfma_f32_32x32x16_bf16 v[32:47], v[88:91], v[84:87], v[32:47]\n" \
  "ds_read_b128 v[116:119], v131 offset:4096\n" \
  "ds_read_b64_tr_b16 v[124:125], v133 offset:28672\n" \
  "ds_read_b64_tr_b16 v[126:127], v133 offset:29696\n" \
  "v_mfma_f32_32x32x16_bf16 v[48:63], v[92:95], v[84:87], v[48:63]\n" \
  "s_add_u32 %[sl], %[sl], 1\n" \
  "s_cmp_eq_u32 %[sl], 4\n" \
  "s_cselect_b32 %[dl], %[wrap], 32768\n" \
  "s_cselect_b32 %[sl], 0, %[sl]\n" \
  "v_mfma_f32_32x32x16_bf16 v[16:31], v[92:95], v[80:83], v[16:31]\n" \
  "v_add_u32 v132, %[dl], v132\n" \
  "v_add_u32 v128, %[dl], v128\n" \
  "v_add_u32 v133, %[dl], v133\n" \
  "v_add_u32 v129, %[dl], v129\n" \
  "v_add_u32 v130, %[dl], v130\n" \
  "v_add_u32 v131, %[dl], v131\n" \
  "s_waitcnt lgkmcnt(6)\n" \
  "v_mfma_f32_32x32x16_bf16 v[0:15], v[104:107], v[96:99], v[0:15]\n" \
  "v_mfma_f32_32x32x16_bf16 v[32:47], v[104:107], v[100:103], v[32:47]\n" \
  "s_waitcnt lgkmcnt(0)\n" \
  "s_barrier\n" \
  "ds_read_b64_tr_b16 v[72:73], v132 offset:16384\n" \
  "ds_read_b64_tr_b16 v[74:75], v132 offset:17408\n" \
  "ds_read_b128 v[64:67], v128\n" \
  "v_mfma_f32_32x32x16_bf16 v[48:63], v[108:111], v[100:103], v[48:63]\n" \
  "ds_read_b128 v[68:71], v128 offset:4096\n" \
  "ds_read_b64_tr_b16 v[76:77], v133 offset:16384\n" \
  "ds_read_b64_tr_b16 v[78:79], v133 offset:17408\n" \
  "v_mfma_f32_32x32x16_bf16 v[16:31], v[108:111], v[96:99], v[16:31]\n" \
  "s_waitcnt lgkmcnt(6)\n" \
  "v_mfma_f32_32x32x16_bf16 v[0:15], v[120:123], v[112:115], v[0:15]\n" \
  "ds_read_b64 v[136:137], v152\n" \
  "ds_read_b64 v[140:141], v152 offset:256\n" \
  "ds_read_b64 v[144:145], v153\n" \
  "ds_read_b64 v[148:149], v153 offset:256\n" \
  "ds_read_b64_tr_b16 v[88:89], v132 offset:20480\n" \
  "v_mfma_f32_32x32x16_bf16 v[32:47], v[120:123], v[116:119], v[32:47]\n" \
  "ds_read_b64_tr_b16 v[90:91], v132 offset:21504\n" \
  "ds_read_b128 v[80:83], v129\n" \
  "ds_read_b128 v[84:87], v129 offset:4096\n" \
  "ds_read_b64_tr_b16 v[92:93], v133 offset:20480\n" \
  "ds_read_b64_tr_b16 v[94:95], v133 offset:21504\n" \
  "v_mfma_f32_32x32x16_bf16 v[48:63], v[124:127], v[116:119], v[48:63]\n" \
  "s_sub_u32 %[it], %[it], 1\n" \
  "s_cmp_lg_u32 %[it], 0\n" \
  "v_mfma_f32_32x32x16_bf16 v[16:31], v[124:127], v[112:115], v[16:31]\n" \
  "s_cbranch_scc1 .Lg_%=\n" \
  "s_waitcnt lgkmcnt(0)\n" \
  "ds_read_b32 v64, v154 offset:0\n" \
  "ds_read_b32 v65, v154 offset:128\n" \
  "v_mfma_f32_32x32x16_bf16 v[0:15], v[144:147], v[136:139], v[0:15]\n" \
  "v_mfma_f32_32x32x16_bf16 v[16:31], v[148:151], v[136:139], v[16:31]\n" \
  "v_mfma_f32_32x32x16_bf16 v[32:47], v[144:147], v[140:143], v[32:47]\n" \
  "v_mfma_f32_32x32x16_bf16 v[48:63], v[148:151], v[140:143], v[48:63]\n" \
  "s_waitcnt lgkmcnt(0)\n" \
  "s_nop 10\n" \
  ""
#define GEMM_ASM_V \
  "v_mov_b32 v0, 0\n" \
  "v_mov_b32 v1, 0\n" \
  "v_mov_b32 v2, 0\n" \
  "v_mov_b32 v3, 0\n" \
  "v_mov_b32 v4, 0\n" \
  "v_mov_b32 v5, 0\n" \
  "v_mov_b32 v6, 0\n" \
  "v_mov_b32 v7, 0\n" \
  "v_mov_b32 v8, 0\n" \
  "v_mov_b32 v9, 0\n" \
  "v_mov_b32 v10, 0\n" \
  "v_mov_b32 v11, 0\n" \
  "v_mov_b32 v12, 0\n" \
  "v_mov_b32 v13, 0\n" \
  "v_mov_b32 v14, 0\n" \
  "v_mov_b32 v15, 0\n" \
  "v_mov_b32 v16, 0\n" \
  "v_mov_b32 v17, 0\n" \
  "v_mov_b32 v18, 0\n" \
  "v_mov_b32 v19, 0\n" \
  "v_mov_b32 v20, 0\n" \
  "v_mov_b32 v21, 0\n" \
  "v_mov_b32 v22, 0\n" \
  "v_mov_b32 v23, 0\n" \
  "v_mov_b32 v24, 0\n" \
  "v_mov_b32 v25, 0\n" \
  "v_mov_b32 v26, 0\n" \
  "v_mov_b32 v27, 0\n" \
  "v_mov_b32 v28, 0\n" \
  "v_mov_b32 v29, 0\n" \
  "v_mov_b32 v30, 0\n" \
  "v_mov_b32 v31, 0\n" \
  "v_mov_b32 v32, 0\n" \
  "v_mov_b32 v33, 0\n" \
  "v_mov_b32 v34, 0\n" \
  "v_mov_b32 v35, 0\n" \
  "v_mov_b32 v36, 0\n" \
  "v_mov_b32 v37, 0\n" \
  "v_mov_b32 v38, 0\n" \
  "v_mov_b32 v39, 0\n" \
  "v_mov_b32 v40, 0\n" \
  "v_mov_b32 v41, 0\n" \
  "v_mov_b32 v42, 0\n" \
  "v_mov_b32 v43, 0\n" \
  "v_mov_b32 v44, 0\n" \
  "v_mov_b32 v45, 0\n" \
  "v_mov_b32 v46, 0\n" \
  "v_mov_b32 v47, 0\n" \
  "v_mov_b32 v48, 0\n" \
  "v_mov_b32 v49, 0\n" \
  "v_mov_b32 v50, 0\n" \
  "v_mov_b32 v51, 0\n" \
  "v_mov_b32 v52, 0\n" \
  "v_mov_b32 v53, 0\n" \
  "v_mov_b32 v54, 0\n" \
  "v_mov_b32 v55, 0\n" \
  "v_mov_b32 v56, 0\n" \
  "v_mov_b32 v57, 0\n" \
  "v_mov_b32 v58, 0\n" \
  "v_mov_b32 v59, 0\n" \
  "v_mov_b32 v60, 0\n" \
  "v_mov_b32 v61, 0\n" \
  "v_mov_b32 v62, 0\n" \
  "v_mov_b32 v63, 0\n" \
  "v_mov_b32 v138, 0\n" \
  "v_mov_b32 v139, 0\n" \
  "v_mov_b32 v142, 0\n" \
  "v_mov_b32 v143, 0\n" \
  "v_mov_b32 v146, 0\n" \
  "v_mov_b32 v147, 0\n" \
  "v_mov_b32 v150, 0\n" \
  "v_mov_b32 v151, 0\n" \
  "s_barrier\n" \
  "ds_read_b64_tr_b16 v[72:73], v132 offset:16384\n" \
  "ds_read_b64_tr_b16 v[74:75], v132 offset:17408\n" \
  "ds_read_b128 v[64:67], v128\n" \
  "ds_read_b128 v[68:71], v128 offset:4096\n" \
  "ds_read_b64_tr_b16 v[76:77], v133 offset:16384\n" \
  "ds_read_b64_tr_b16 v[78:79], v133 offset:17408\n" \
  "ds_read_b64 v[136:137], v152\n" \
  "ds_read_b64 v[140:141], v152 offset:256\n" \
  "ds_read_b64 v[144:145], v153\n" \
  "ds_read_b64 v[148:149], v153 offset:256\n" \
  "ds_read_b64_tr_b16 v[88:89], v132 offset:20480\n" \
  "ds_read_b64_tr_b16 v[90:91], v132 offset:21504\n" \
  "ds_read_b128 v[80:83], v129\n" \
  "ds_read_b128 v[84:87], v129 offset:4096\n" \
  "ds_read_b64_tr_b16 v[92:93], v133 offset:20480\n" \
  "ds_read_b64_tr_b16 v[94:95], v133 offset:21504\n" \
  ".Lg_%=:\n" \
  "s_waitcnt lgkmcnt(10)\n" \
  "v_mfma_f32_32x32x16_bf16 v[0:15], v[64:67], v[72:75], v[0:15]\n" \
  "ds_read_b64_tr_b16 v[104:105], v132 offset:24576\n" \
  "ds_read_b64_tr_b16 v[106:107], v132 offset:25600\n" \
  "ds_read_b128 v[96:99], v130\n" \
  "v_mfma_f32_32x32x16_bf16 v[32:47], v[68:71], v[72:75], v[32:47]\n" \
  "ds_read_b128 v[100:103], v130 offset:4096\n" \
  "ds_read_b64_tr_b16 v[108:109], v133 offset:24576\n" \
  "ds_read_b64_tr_b16 v[110:111], v133 offset:25600\n" \
  "v_mfma_f32_32x32x16_bf16 v[48:63], v[68:71], v[76:79], v[48:63]\n" \
  "v_mfma_f32_32x32x16_bf16 v[16:31], v[64:67], v[76:79], v[16:31]\n" \
  "s_waitcnt lgkmcnt(6)\n" \
  "v_mfma_f32_32x32x16_bf16 v[0:15], v[80:83], v[88:91], v[0:15]\n" \
  "ds_read_b64_tr_b16 v[120:121], v132 offset:28672\n" \
  "ds_read_b64_tr_b16 v[122:123], v132 offset:29696\n" \
  "ds_read_b128 v[112:115], v131\n" \
  "v_mfma_f32_32x32x16_bf16 v[32:47], v[84:87], v[88:91], v[32:47]\n" \
  "ds_read_b128 v[116:119], v131 offset:4096\n" \
  "ds_read_b64_tr_b16 v[124:125], v133 offset:28672\n" \
  "ds_read_b64_tr_b16 v[126:127], v133 offset:29696\n" \
  "v_mfma_f32_32x32x16_bf16 v[48:63], v[84:87], v[92:95], v[48:63]\n" \
  "s_add_u32 %[sl], %[sl], 1\n" \
  "s_cmp_eq_u32 %[sl], 4\n" \
  "s_cselect_b32 %[dl], %[wrap], 32768\n" \
  "s_cselect_b32 %[sl], 0, %[sl]\n" \
  "v_mfma_f32_32x32x16_bf16 v[16:31], v[80:83], v[92:95], v[16:31]\n" \
  "v_add_u32 v132, %[dl], v132\n" \
  "v_add_u32 v128, %[dl], v128\n" \
  "v_add_u32 v133, %[dl], v133\n" \
  "v_add_u32 v129, %[dl], v129\n" \
  "v_add_u32 v130, %[dl], v130\n" \
  "v_add_u32 v131, %[dl], v131\n" \
  "s_waitcnt lgkmcnt(6)\n" \
  "v_mfma_f32_32x32x16_bf16 v[0:15], v[96:99], v[104:107], v[0:15]\n" \
  "v_mfma_f32_32x32x16_bf16 v[32:47], v[100:103], v[104:107], v[32:47]\n" \
  "s_waitcnt lgkmcnt(0)\n" \
  "s_barrier\n" \
  "ds_read_b64_tr_b16 v[72:73], v132 offset:16384\n" \
  "ds_read_b64_tr_b16 v[74:75], v132 offset:17408\n" \
  "ds_read_b128 v[64:67], v128\n" \
  "v_mfma_f32_32x32x16_bf16 v[48:63], v[100:103], v[108:111], v[48:63]\n" \
  "ds_read_b128 v[68:71], v128 offset:4096\n" \
  "ds_read_b64_tr_b16 v[76:77], v133 offset:16384\n" \
  "ds_read_b64_tr_b16 v[78:79], v133 offset:17408\n" \
  "v_mfma_f32_32x32x16_bf16 v[16:31], v[96:99], v[108:111], v[16:31]\n" \
  "s_waitcnt lgkmcnt(6)\n" \
  "v_mfma_f32_32x32x16_bf16 v[0:15], v[112:115], v[120:123], v[0:15]\n" \
  "ds_read_b64 v[136:137], v152\n" \
  "ds_read_b64 v[140:141], v152 offset:256\n" \
  "ds_read_b64 v[144:145], v153\n" \
  "ds_read_b64 v[148:149], v153 offset:256\n" \
  "ds_read_b64_tr_b16 v[88:89], v132 offset:20480\n" \
  "v_mfma_f32_32x32x16_bf16 v[32:47], v[116:119], v[120:123], v[32:47]\n" \
  "ds_read_b64_tr_b16 v[90:91], v132 offset:21504\n" \
  "ds_read_b128 v[80:83], v129\n" \
  "ds_read_b128 v[84:87], v129 offset:4096\n" \
  "ds_read_b64_tr_b16 v[92:93], v133 offset:20480\n" \
  "ds_read_b64_tr_b16 v[94:95], v133 offset:21504\n" \
  "v_mfma_f32_32x32x16_bf16 v[48:63], v[116:119], v[124:127], v[48:63]\n" \
  "s_sub_u32 %[it], %[it], 1\n" \
  "s_cmp_lg_u32 %[it], 0\n" \
  "v_mfma_f32_32x32x16_bf16 v[16:31], v[112:115], v[124:127], v[16:31]\n" \
  "s_cbranch_scc1 .Lg_%=\n" \
  "s_waitcnt lgkmcnt(0)\n" \
  "ds_read_b128 v[64:67], v154 offset:0\n" \
  "ds_read_b128 v[68:71], v154 offset:32\n" \
  "ds_read_b128 v[72:75], v154 offset:64\n" \
  "ds_read_b128 v[76:79], v154 offset:96\n" \
  "ds_read_b128 v[80:83], v154 offset:128\n" \
  "ds_read_b128 v[84:87], v154 offset:160\n" \
  "ds_read_b128 v[88:91], v154 offset:192\n" \
  "ds_read_b128 v[92:95], v154 offset:224\n" \
  "v_mfma_f32_32x32x16_bf16 v[0:15], v[136:139], v[144:147], v[0:15]\n" \
  "v_mfma_f32_32x32x16_bf16 v[16:31], v[136:139], v[148:151], v[16:31]\n" \
  "v_mfma_f32_32x32x16_bf16 v[32:47], v[140:143], v[144:147], v[32:47]\n" \
  "v_mfma_f32_32x32x16_bf16 v[48:63], v[140:143], v[148:151], v[48:63]\n" \
  "s_waitcnt lgkmcnt(0)\n" \
  "s_nop 10\n" \
  ""

constexpr int S = 2048, D = 640, F = 1536;
constexpr int SPIN_CAP = 1 << 16;
constexpr int SC1 = 16;
constexpr float QSCALE = 0.22360679774997896f * 1.4426950408889634f;

constexpr unsigned MAT = S * D * 2;
constexpr unsigned WS_Q = 0;
constexpr unsigned WS_K = MAT;
constexpr unsigned WS_V = 2 * MAT;
constexpr unsigned WS_O = 3 * MAT;
constexpr unsigned WS_EPOCH = 4 * MAT;
constexpr unsigned WS_STG = WS_EPOCH + 256;
constexpr unsigned WS_CSG = WS_STG + S * 16;
constexpr unsigned WS_SEL = WS_CSG + 1920 * 16;
constexpr unsigned WS_XS = WS_SEL + 256;
constexpr unsigned WS_PU = WS_XS + 10240;
constexpr unsigned WS_H = WS_PU + 960 * 1024;
constexpr unsigned WS_Y = WS_H + 24576;
constexpr unsigned WS_F1 = WS_Y + 81920;
constexpr unsigned WS_F2 = WS_F1 + 960 * 128;
constexpr unsigned WS_F3 = WS_F2 + 1024 * 128;
constexpr unsigned WS_FS = WS_F3 + 1024 * 128;
constexpr unsigned WS_FU = WS_FS + 4 * 128;
constexpr unsigned WS_FH = WS_FU + 240 * 128;
constexpr unsigned WS_FY = WS_FH + 96 * 128;
constexpr unsigned WS_END = WS_FY + 160 * 128;

constexpr int NSLOT = 4;
constexpr int SL = 10496;
constexpr int L_ACC = 2 * 10496;
constexpr int L_WO = 43008;
constexpr int L_MISC = 145408;
constexpr int L_KV = L_MISC;
constexpr int L_QF = L_MISC + 16;
constexpr int L_XF = L_MISC + 24;
constexpr int L_TAG = L_MISC + 80;
constexpr int L_EX = 147456;
constexpr int L_ST = L_EX + 12288;
constexpr int LDS_BYTES = 163840;
constexpr unsigned L_OOB = 0x40000;

#define TMARK(k)
#define CMARK(k)
#define TDUMP()

extern __shared__ __attribute__((aligned(16))) char smem[];

struct Ctx {
  const void *x, *wqkv, *wo, *wr, *wg, *wu, *wd;
  char* ws; void* out;
  rsrc_t rws;
  unsigned tagA, tagB;
  u32x2 vtag; unsigned v0;
  int wid, lane, bid;
};

#define PIN(x) asm volatile("" : "+v"(x))
#define ISSUED() __builtin_amdgcn_sched_barrier(0)
DI rsrc_t mk_rsrc(const void* p, unsigned bytes) { return __builtin_amdgcn_make_buffer_rsrc((void*)p, 0, bytes, 0x00020000); }
DI u32x4 ld128(rsrc_t r, unsigned v, unsigned s, int aux) { return aux ? __builtin_amdgcn_raw_buffer_load_b128(r, v, s, SC1) : __builtin_amdgcn_raw_buffer_load_b128(r, v, s, 0); }
DI void st128(rsrc_t r, u32x4 d, unsigned v) { __builtin_amdgcn_raw_buffer_store_b128(d, r, v, 0, SC1); }
DI void st128x(rsrc_t r, u32x4 d, unsigned v) { __builtin_amdgcn_raw_buffer_store_b128(d, r, v, 0, (0 & 2) ? 0 : (0 & 4) ? 18 : SC1); }
DI void st64(rsrc_t r, u32x2 d, unsigned v) { __builtin_amdgcn_raw_buffer_store_b64(d, r, v, 0, SC1); }
DI unsigned cvt_pk(float lo, float hi) {
  typedef __attribute__((ext_vector_type(2))) float f2; typedef __attribute__((ext_vector_type(2))) __bf16 b2;
  return __builtin_bit_cast(unsigned, __builtin_convertvector((f2){lo, hi}, b2));
}
DI float bf_lo(unsigned u) { return __uint_as_float(u << 16); }
DI float bf_hi(unsigned u) { return __uint_as_float(u & 0xffff0000u); }
DI float rbf(float x) { return bf_lo(cvt_pk(x, x)); }
DI float swap32(float x) {
  const auto q = __builtin_amdgcn_permlane32_swap(__float_as_uint(x), __float_as_uint(x), false, false);
  return __uint_as_float((threadIdx.x & 32) ? q[0] : q[1]);
}
DI float swap16(float x) {
  const auto q = __builtin_amdgcn_permlane16_swap(__float_as_uint(x), __float_as_uint(x), false, false);
  return __uint_as_float((threadIdx.x & 16) ? q[0] : q[1]);
}
template <int CTRL> DI unsigned dpp(unsigned v) { return __builtin_amdgcn_update_dpp(v, v, CTRL, 0xf, 0xf, true); }
DI float sum8(float v) {
  v += __uint_as_float(dpp<0xB1>(__float_as_uint(v)));
  v += __uint_as_float(dpp<0x4E>(__float_as_uint(v)));
  v += __uint_as_float(dpp<0x141>(__float_as_uint(v)));
  return v;
}
DI unsigned wave_max(unsigned v) {
  v = max(v, dpp<0xB1>(v)); v = max(v, dpp<0x4E>(v)); v = max(v, dpp<0x141>(v)); v = max(v, dpp<0x140>(v));
  v = max(v, __float_as_uint(swap16(__uint_as_float(v))));
  return max(v, __float_as_uint(swap32(__uint_as_float(v))));
}
#define DMA_ASM(op) asm volatile("s_mov_b32 m0, %0\n\ts_nop 0\n\t" op \
               ::"s"(__builtin_amdgcn_readfirstlane(lds)), "v"(voff), "s"(rs), "s"(__builtin_amdgcn_readfirstlane(soff)) : "memory")
template <int POL = 0> DI void dma16(rsrc_t rs, unsigned voff, unsigned soff, unsigned lds) {
  if (POL == 0) DMA_ASM("buffer_load_dwordx4 %1, %2, %3 offen lds");
  if (POL == 1) DMA_ASM("buffer_load_dwordx4 %1, %2, %3 offen sc1 lds");
  if (POL == 2) DMA_ASM("buffer_load_dwordx4 %1, %2, %3 offen nt lds");
}
DI void dma4(rsrc_t rs, unsigned voff, unsigned soff, unsigned lds) { DMA_ASM("buffer_load_dword %1, %2, %3 offen sc1 lds"); }
DI void dma16_masked(rsrc_t rs, unsigned voff, unsigned soff, unsigned lds, unsigned long long lanes) {
  asm volatile("s_mov_b32 m0, %0\n\ts_mov_b64 exec, %4\n\tbuffer_load_dwordx4 %1, %2, %3 offen sc1 lds\n\ts_mov_b64 exec, -1"
               ::"s"(__builtin_amdgcn_readfirstlane(lds)), "v"(voff), "s"(rs), "s"(__builtin_amdgcn_readfirstlane(soff)), "s"(lanes) : "memory");
}
template <int N> DI void wait_vm() { asm volatile("s_waitcnt vmcnt(%0)" ::"n"(N) : "memory"); }
DI void wait_lds() { asm volatile("s_waitcnt lgkmcnt(0)" ::: "memory"); }
DI void wg_barrier() { asm volatile("s_waitcnt lgkmcnt(0)\n\ts_barrier" ::: "memory"); }
DI void fence() { asm volatile("" ::: "memory"); }
DI unsigned lds_ld(unsigned off) { return *(volatile LAS unsigned*)(uintptr_t)off; }
DI void lds_st(unsigned off, unsigned v) { *(volatile LAS unsigned*)(uintptr_t)off = v; }
template <int SLEEP> DI void lds_wait(unsigned off, unsigned mask, unsigned need) {
#pragma unroll 1
  for (int i = 0; i < SPIN_CAP && __any((lds_ld(off) & mask) < need); ++i) __builtin_amdgcn_s_sleep(SLEEP);
  fence();
}
template <int SLEEP> DI void lds_wait1(unsigned off, unsigned mask, unsigned need) {
#pragma unroll 1
  for (int i = 0; i < SPIN_CAP && (__builtin_amdgcn_readfirstlane(lds_ld(off)) & mask) < need; ++i) __builtin_amdgcn_s_sleep(SLEEP);
  fence();
}
DI void flag_prep(Ctx& p) { p.vtag = (u32x2){p.tagA, p.tagB}; p.v0 = p.lane == 0 ? 0 : WS_END; PIN(p.vtag); PIN(p.v0); }
DI void flag_set(const Ctx& p, unsigned off) { wait_vm<0>(); __builtin_amdgcn_raw_buffer_store_b64(p.vtag, p.rws, p.v0, off, SC1); }
DI unsigned long long flag_misses(const Ctx& p, unsigned voff, unsigned soff) {
  fence();
  const u32x2 v = __builtin_amdgcn_raw_buffer_load_b64(p.rws, voff, soff, SC1);
  return __ballot(__builtin_bit_cast(unsigned long long, v) != (p.tagA | (unsigned long long)p.tagB << 32));
}
template <int SLEEP> DI void flag_wait(const Ctx& p, unsigned voff, unsigned soff = 0) {
#pragma unroll 1
  for (int i = 0; i < SPIN_CAP && flag_misses(p, voff, soff); ++i) __builtin_amdgcn_s_sleep(SLEEP);
  fence();
}
template <int N> DI void granule_wait(const Ctx& p, unsigned off, unsigned stride, u32x4 (&g)[N]) {
#pragma unroll 1
  for (int i = 0; i < SPIN_CAP; ++i) {
    fence();
    bool ok = true;
#pragma unroll
    for (int k = 0; k < N; ++k) g[k] = ld128(p.rws, off, k * stride, SC1);
#pragma unroll
    for (int k = 0; k < N; ++k) ok &= g[k][1] == p.tagA && g[k][3] == p.tagB;
    if (__all(ok)) break;
    __builtin_amdgcn_s_sleep(2);
  }
  fence();
}
constexpr unsigned MARK = 0xffffffffu;
DI unsigned umax4(const u32x4& v) { return max(max(v[0], v[1]), max(v[2], v[3])); }
DI unsigned nomark(unsigned v) { return min(v, MARK - 1); }
DI float ln_sd(float var) { return rbf(__fsqrt_rn(rbf(rbf(var) + 1e-5f))); }
DI float ln_norm(float x, float m, float rs) { return rbf(rbf(x - m) * rs); }
DI float sigmoid(float z) { return __builtin_amdgcn_rcpf(1.0f + __builtin_amdgcn_exp2f(z * -1.4426950408889634f)); }
DI float unpk(const u32x4& v, int k) { return (k & 1) ? bf_hi(v[k >> 1]) : bf_lo(v[k >> 1]); }

DI int col_half(int n, int second) { return n + 5 * (((n + 6) >> 4) + second * (2 + ((n + 11) >> 4) - ((n + 6) >> 4))); }
struct Feed { rsrc_t rx, rw; unsigned vx0, vx1, vw; int rot; };
DI void feed_init(const Ctx& p, int m, int n, int rot, Feed& f) {
  const int lane = p.lane, lw = p.wid & 3, u = ((lane >> 2) & 3) ^ (lane >> 4);
  const unsigned row = (m * 128 + lw * 32 + (lane >> 3)) * 1280;
  f.vx0 = row + (((lane & 7) ^ (lane >> 4)) << 4); f.vx1 = row + (((lane & 7) ^ (4 + (lane >> 4))) << 4);
  f.vw = (lw * 16 + (lane >> 4)) * 3840 + col_half(n, u >> 1) * 128 + ((u & 1) << 6) + ((lane & 3) << 4);
  f.rot = rot;
  PIN(f.vx0); PIN(f.vx1); PIN(f.vw); ISSUED();
  f.rx = mk_rsrc(p.x, n < 15 ? MAT : 0); f.rw = mk_rsrc(p.wqkv, n < 15 ? 3 * D * D * 2 : 0);
}
template <int N> DI void qkv_issue(const Feed& f, int lw, int i0, int k) {
  const unsigned slot = (k % NSLOT) * 32768 + lw * 4096;
  const int c = k + f.rot >= 10 ? k + f.rot - 10 : k + f.rot;
#pragma unroll
  for (int j = 0; j < N; ++j) {
    const int i = i0 + j;
    dma16<0>(f.rx, (j & 1) ? f.vx1 : f.vx0, c * 128 + i * 8 * 1280, slot + i * 1024);
    dma16<0>(f.rw, f.vw, (c * 64 + i * 4) * 3840, slot + 16384 + i * 1024);
  }
}
DI void read_tags(Ctx& p) {
  p.tagA = __builtin_amdgcn_readfirstlane(lds_ld(L_TAG)); p.tagB = __builtin_amdgcn_readfirstlane(lds_ld(L_TAG + 4));
  flag_prep(p);
}
DI void bar() { asm volatile("s_barrier" ::: "memory"); }
template <int N> DI void wait_vm_bar() { asm volatile("s_waitcnt vmcnt(%0)\n\ts_barrier" ::"n"(N) : "memory"); }

struct Gate { u32x2 x[5]; u32x4 st; };
DI void qkv_dma(Ctx& p, const Feed& f, int m, int n) {
  const int lane = p.lane, lw = p.wid - 4;
  unsigned vg = lane < 32 ? WS_STG + (m * 128 + lw * 32 + lane) * 16 : WS_CSG + (col_half(n, lw >> 1) * 64 + (lw & 1) * 32 + (lane & 31)) * 16;
  unsigned la = L_ST + (lane >> 5) * 1024 + (lw * 32 + (lane & 31)) * 8, lb = lane < 32 ? L_ST + 2048 + (lw * 32 + lane) * 4 : L_OOB;
#pragma unroll 1
  for (int k = 0; k < 9; ++k) {
    qkv_issue<4>(f, lw, 0, k + 1);
    wait_vm_bar<8>();
    if (k == 7) { read_tags(p); PIN(vg); PIN(la); PIN(lb); }
    TMARK(2 + k);
  }
  u32x4 tg[1];
  asm volatile("s_nop 4\n\tbuffer_load_dwordx4 %0, %1, %2, 0 offen sc1\n\ts_waitcnt vmcnt(1)\n\ts_barrier\n\ts_waitcnt vmcnt(0)"
               : "=&v"(tg[0]) : "v"(vg), "s"(p.rws) : "memory");
  TMARK(11);
  if (__builtin_expect(!__all(tg[0][1] == p.tagA && tg[0][3] == p.tagB), 0)) granule_wait<1>(p, vg, 0, tg);
  const unsigned nm = (tg[0][0] & 0xffffu) ^ 0x8000u;
  *(LAS u32x2*)(uintptr_t)la = (u32x2){lane < 32 ? nm * 0x10001u : tg[0][0], lane < 32 ? nm : tg[0][2]};
  *(LAS float*)(uintptr_t)lb = __builtin_amdgcn_rcpf(bf_hi(tg[0][0]));
  wg_barrier();
}

DI float sum32(float v) { v = sum8(v); v += __uint_as_float(dpp<0x140>(__float_as_uint(v))); return v + swap16(v); }
struct RowData { u32x4 x0, x1, w0, w1; u32x2 x2, w2, ep; };
DI void row_load(const Ctx& p, int row0, RowData& d) {
  const int lane = p.lane, row = row0 + (lane >> 5), sub = lane & 31;
  const rsrc_t rx = mk_rsrc(p.x, MAT), rr = mk_rsrc(p.wr, D * 2), re = mk_rsrc(p.ws, WS_END);
  asm volatile("s_nop 4\n\tbuffer_load_dwordx4 %0, %7, %11, 0 offen\n\tbuffer_load_dwordx4 %1, %7, %11, 0 offen offset:512\n\tbuffer_load_dwordx2 %2, %8, %11, 0 offen offset:1024\n\t"
               "buffer_load_dwordx4 %3, %9, %12, 0 offen\n\tbuffer_load_dwordx4 %4, %9, %12, 0 offen offset:512\n\tbuffer_load_dwordx2 %5, %10, %12, 0 offen offset:1024\n\t"
               "buffer_load_dwordx2 %6, off, %13, %14"
               : "=&v"(d.x0), "=&v"(d.x1), "=&v"(d.x2), "=&v"(d.w0), "=&v"(d.w1), "=&v"(d.w2), "=&v"(d.ep)
               : "v"(row * 1280 + sub * 16), "v"(row * 1280 + sub * 8), "v"(sub * 16), "v"(sub * 8), "s"(rx), "s"(rr), "s"(re), "s"(WS_EPOCH) : "memory");
}
DI void row_stats(Ctx& p, int row0, RowData& rd) {
  const int lane = p.lane, row = row0 + (lane >> 5), sub = lane & 31;
  asm volatile("s_waitcnt vmcnt(%7)" : "+v"(rd.x0), "+v"(rd.x1), "+v"(rd.x2), "+v"(rd.w0), "+v"(rd.w1), "+v"(rd.w2), "+v"(rd.ep) : "n"(4));
  const u32x2 ep = rd.ep;
  const unsigned xu[10] = {rd.x0[0], rd.x0[1], rd.x0[2], rd.x0[3], rd.x1[0], rd.x1[1], rd.x1[2], rd.x1[3], rd.x2[0], rd.x2[1]};
  const unsigned wu[10] = {rd.w0[0], rd.w0[1], rd.w0[2], rd.w0[3], rd.w1[0], rd.w1[1], rd.w1[2], rd.w1[3], rd.w2[0], rd.w2[1]};
  typedef __attribute__((ext_vector_type(2))) float f2;
  f2 s = {0.f, 0.f}, q = s, d = s, ws = s;
#pragma unroll
  for (int j = 0; j < 10; ++j) {
    const f2 a = {bf_lo(xu[j]), bf_hi(xu[j])}, w = {bf_lo(wu[j]), bf_hi(wu[j])};
    s += a; q += a * a; d += a * w; ws += w;
  }
  const float mean = sum32(s[0] + s[1]) * (1.0f / D), dot = sum32(d[0] + d[1]), wsum = sum32(ws[0] + ws[1]);
  float var = sum32(q[0] + q[1]) * (1.0f / D) - mean * mean;
  if (__builtin_expect(__any(mean * mean > 64.0f * var), 0)) {
    q = (f2){0.f, 0.f};
#pragma unroll
    for (int j = 0; j < 10; ++j) { const f2 a = (f2){bf_lo(xu[j]), bf_hi(xu[j])} - mean; q += a * a; }
    var = sum32(q[0] + q[1]) * (1.0f / D);
  }
  const float mb = rbf(mean), sd = ln_sd(var), r = rbf((dot - mb * wsum) * __builtin_amdgcn_rcpf(sd));
  p.tagA = __builtin_amdgcn_readfirstlane(ep[0]) + 1; p.tagB = __builtin_amdgcn_readfirstlane(ep[1]) + 1;
  if (sub == 0) st128(p.rws, (u32x4){cvt_pk(mb, sd), p.tagA, __float_as_uint(r), p.tagB}, WS_STG + row * 16);
  if (p.wid == 0) { lds_st(L_TAG, p.tagA); lds_st(L_TAG + 4, p.tagB); }
  flag_prep(p);
}

DI unsigned peek(const void* q, unsigned off) { unsigned v; asm volatile("s_load_dword %0, %1, %2" : "=s"(v) : "s"(q), "s"(off)); return v; }
DI void touch_pages(const Ctx& p) {
  unsigned v[16];
#pragma unroll
  for (int i = 0; i < 6; ++i) v[i] = peek(p.ws, i << 21);
  v[6] = peek(p.out, 0); v[7] = peek(p.out, MAT - 4); v[8] = peek(p.wo, 0); v[9] = peek(p.wo, D * D * 2 - 4);
  v[10] = peek(p.wg, 0); v[11] = peek(p.wg, D * F * 2 - 4); v[12] = peek(p.wu, 0); v[13] = peek(p.wu, D * F * 2 - 4);
  v[14] = peek(p.wd, 0); v[15] = peek(p.wd, D * F * 2 - 4);
  asm volatile("s_waitcnt lgkmcnt(0)" : "+s"(v[0]), "+s"(v[1]), "+s"(v[2]), "+s"(v[3]), "+s"(v[4]), "+s"(v[5]), "+s"(v[6]), "+s"(v[7]), "+s"(v[8]),
               "+s"(v[9]), "+s"(v[10]), "+s"(v[11]), "+s"(v[12]), "+s"(v[13]), "+s"(v[14]), "+s"(v[15]));
}

#define C10(a) "v" #a "0","v" #a "1","v" #a "2","v" #a "3","v" #a "4","v" #a "5","v" #a "6","v" #a "7","v" #a "8","v" #a "9"

#define GEMM_CLOB "v96","v97","v98","v99",C10(10),C10(11),"v120","v121","v122","v123","v124","v125","v126","v127","v136","v137","v138","v139",C10(14),"v150","v151","scc","memory"

template <bool VT> DI void qkv_mfma(Ctx& p, int m, int ch) {
  const int lane = p.lane, wm = p.wid >> 1, wn = p.wid & 1, r = lane & 31, hh = lane >> 5;
  u32x4 ax; u32x2 aw;
  const int row = wm * 64 + r, q = (lane & 15) >> 2;
#pragma unroll
  for (int s = 0; s < 4; ++s) ax[s] = row * 128 + (((2 * s + hh) ^ ((row >> 1) & 7)) << 4);
#pragma unroll
  for (int f = 0; f < 2; ++f) aw[f] = (8 * hh + q) * 256 + (((wn * 2 + f) ^ q) << 6) + (((lane >> 4) & 1) << 5) + ((lane & 3) << 3);
  const unsigned aM = hh ? L_OOB : L_ST + (wm * 64 + r) * 8, aC = L_ST + 1024 + (wn * 64 + r) * 8;
  const unsigned aR = L_ST + 2048 + (wm * 64 + (VT ? 4 * hh : r)) * 4;
  f32x16 a00, a01, a10, a11, rs0, rs1;
  int it = 10, sl = 0, dl;
#define GEMM_OPS : "={v[0:15]}"(a00), "={v[16:31]}"(a01), "={v[32:47]}"(a10), "={v[48:63]}"(a11), "={v[64:79]}"(rs0), "={v[80:95]}"(rs1), \
      "+{v[128:131]}"(ax), "+{v[132:133]}"(aw), [it] "+s"(it), [sl] "+s"(sl), [dl] "=&s"(dl) \
    : "{v152}"(aM), "{v153}"(aC), "{v154}"(aR), [wrap] "s"(-(NSLOT - 1) * 32768) : GEMM_CLOB
  if constexpr (VT) asm volatile(GEMM_ASM_V GEMM_OPS); else asm volatile(GEMM_ASM_QK GEMM_OPS);
  TMARK(2);
  const unsigned base = VT ? WS_V + (((m * 4 + wm * 2) * 4 + hh) * 640 + (ch - 20) * 64 + r) * 16
                           : WS_Q + ((ch * 8 + hh) * 2048 + m * 128 + wm * 64 + r) * 16;
  auto put = [&](const f32x16& a, int i, int j) {
    if constexpr (VT) {
#pragma unroll
      for (int s = 0; s < 2; ++s) {
        const f32x16& rs = i ? rs1 : rs0;
        const f32x4 r0 = {rs[8 * s], rs[8 * s + 1], rs[8 * s + 2], rs[8 * s + 3]}, r1 = {rs[8 * s + 4], rs[8 * s + 5], rs[8 * s + 6], rs[8 * s + 7]};
        const u32x4 v = {cvt_pk(a[8 * s] * r0[0], a[8 * s + 1] * r0[1]), cvt_pk(a[8 * s + 2] * r0[2], a[8 * s + 3] * r0[3]),
                         cvt_pk(a[8 * s + 4] * r1[0], a[8 * s + 5] * r1[1]), cvt_pk(a[8 * s + 6] * r1[2], a[8 * s + 7] * r1[3])};
        st128x(p.rws, v, base + (i * 4 + s * 2) * 10240 + j * 512);
      }
    } else {
      const float rs = rs0[i] * (ch < 10 ? QSCALE : 1.0f);
#pragma unroll
      for (int g = 0; g < 4; g += 2) {
        const auto p0 = __builtin_amdgcn_permlane32_swap(cvt_pk(a[4 * g] * rs, a[4 * g + 1] * rs), cvt_pk(a[4 * g + 4] * rs, a[4 * g + 5] * rs), false, false);
        const auto p1 = __builtin_amdgcn_permlane32_swap(cvt_pk(a[4 * g + 2] * rs, a[4 * g + 3] * rs), cvt_pk(a[4 * g + 6] * rs, a[4 * g + 7] * rs), false, false);
        st128x(p.rws, (u32x4){p0[0], p1[0], p0[1], p1[1]}, base + (j * 4 + g) * 32768 + i * 512);
      }
    }
    ISSUED();
  };
  put(a00, 0, 0); put(a01, 0, 1); put(a10, 1, 0); put(a11, 1, 1);
  TMARK(3);
  flag_set(p, WS_F1 + ((m * 30 + ch) * 2 + wm) * 128);
}

DI void col_sums(Ctx& p, int n) {
  const int lane = p.lane, lw = p.wid - 4;
  const rsrc_t rw = mk_rsrc(p.wqkv, 3 * D * D * 2);
  const unsigned v = (lw * 4 + (lane >> 4)) * 3840 + col_half(n, (lane >> 3) & 1) * 128 + (lane & 7) * 16;
  constexpr int AH = 14;
  u32x4 wv[40];
  f32x4 c0 = {}, c1 = {};
#pragma unroll
  for (int j = -AH; j < 40; ++j) {
    if (j >= 0) {
      c0 += (f32x4){bf_lo(wv[j][0]), bf_hi(wv[j][0]), bf_lo(wv[j][1]), bf_hi(wv[j][1])};
      c1 += (f32x4){bf_lo(wv[j][2]), bf_hi(wv[j][2]), bf_lo(wv[j][3]), bf_hi(wv[j][3])};
    }
    asm volatile("" : "+v"(c0), "+v"(c1)::"memory");
    const int i = j + AH;
    if (i < 40) wv[i] = ld128(rw, v, (((i >> 3) + ((i >> 2) & 1) * 5) * 4 + (i & 3)) * 16 * 3840, 0);
  }
  LAS float* part = (LAS float*)(smem + 32768);
  *(LAS f32x4*)(part + (lw * 64 + lane) * 8) = c0;
  *(LAS f32x4*)(part + (lw * 64 + lane) * 8 + 4) = c1;
  wg_barrier();
  read_tags(p);
  if (lw < 2) {
    float cs = 0.f;
#pragma unroll
    for (int j = 0; j < 16; ++j) cs += part[j * 128 + lw * 64 + lane];
    const float t0 = rbf(cs), t1 = rbf(cs - t0);
    st128(p.rws, (u32x4){cvt_pk(t0, t1), p.tagA, cvt_pk(cs - t0 - t1, 0.f), p.tagB}, WS_CSG + (col_half(n, lw) * 64 + lane) * 16);
  }
  TMARK(12);
  wg_barrier();
}

DI void route(const Ctx& p) {
  const int lane = p.lane, wid = p.wid;
  u32x4 g[4];
  granule_wait<4>(p, WS_STG + (wid * 256 + lane) * 16, 1024, g);
  unsigned key[4];
#pragma unroll
  for (int k = 0; k < 4; ++k) {
    const unsigned u = g[k][2], tok = wid * 256 + k * 64 + lane;
    key[k] = ((u ^ ((u >> 31) ? 0xffffffffu : 0x80000000u)) & 0xffff0000u) | (2047 - tok);
  }
  unsigned mine = 0;
#pragma unroll 1
  for (int t = 0; t < 8; ++t) {
    const unsigned best = wave_max(max(max(key[0], key[1]), max(key[2], key[3])));
#pragma unroll
    for (int k = 0; k < 4; ++k) key[k] = key[k] == best ? 0 : key[k];
    mine = lane == t ? best : mine;
  }
  if (lane < 8) lds_st(L_ST + (wid * 8 + lane) * 4, mine);
  wg_barrier();
  if (wid >= 4) return;
  if (threadIdx.x == 0) st64(p.rws, (u32x2){p.tagA, p.tagB}, WS_EPOCH);
  const unsigned cand = lds_ld(L_ST + lane * 4);
  int rank = 0;
#pragma unroll
  for (int k = 0; k < 16; ++k) {
    const u32x4 o = *(const LAS u32x4*)(smem + L_ST + k * 16);
    rank += (o[0] > cand) + (o[1] > cand) + (o[2] > cand) + (o[3] > cand);
  }
  const rsrc_t rx = mk_rsrc(p.x, MAT);
  const int hh = lane >> 5, t = wid * 2 + hh;
  const unsigned long long b0 = __ballot(rank == wid * 2), b1 = __ballot(rank == wid * 2 + 1);
  const unsigned k0 = __builtin_amdgcn_readlane(cand, __builtin_ctzll(b0 | (1ull << 63))), k1 = __builtin_amdgcn_readlane(cand, __builtin_ctzll(b1 | (1ull << 63)));
  const int tok = 2047 - (int)((hh ? k1 : k0) & 0x7ffu);
  const rsrc_t rr = mk_rsrc(p.wr, D * 2);
  u32x4 v[3], w[3];
#pragma unroll
  for (int j = 0; j < 3; ++j) { v[j] = ld128(rx, tok * 1280 + min(j * 32 + (lane & 31), 79) * 16, 0, 0); w[j] = ld128(rr, (j * 32 + (lane & 31)) * 16, 0, 0); }
  const unsigned sg = __builtin_amdgcn_raw_buffer_load_b32(p.rws, WS_STG + tok * 16, 0, SC1);
  const float mb = bf_lo(sg), rs = __builtin_amdgcn_rcpf(bf_hi(sg));
  float rv = 0.f;
#pragma unroll
  for (int j = 0; j < 3; ++j) {
    u32x4 o;
#pragma unroll
    for (int k = 0; k < 4; ++k) {
      o[k] = cvt_pk(ln_norm(bf_lo(v[j][k]), mb, rs), ln_norm(bf_hi(v[j][k]), mb, rs));
      rv = fmaf(bf_lo(o[k]), bf_lo(w[j][k]), fmaf(bf_hi(o[k]), bf_hi(w[j][k]), rv));
    }
    st128(p.rws, o, WS_XS + (min(j * 32 + (lane & 31), 79) * 8 + t) * 16);
  }
  rv = rbf(sum32(rv));
  if ((lane & 31) == 0) st64(p.rws, (u32x2){(unsigned)tok, __float_as_uint(rbf(sigmoid(rv)))}, WS_SEL + t * 8);
  flag_set(p, WS_FS + wid * 128);
}

DI void attn_exact(const Ctx& p, int h, int tok0, f32x4 (&o)[3], float& lout) {
  const int lane = p.lane, r = lane & 31, hh = lane >> 5, odd = h & 1, oa = (h >> 1) * 5 + 3 * odd, ob = (h >> 1) * 5 + 2;
  float q[20], acc[20], mx = -INFINITY, l = 0.f;
  auto row20 = [&](unsigned base, int tok, float* dst) {
    const u32x4 a = ld128(p.rws, base + (oa * 2048 + tok) * 16, 0, SC1), c = ld128(p.rws, base + ((oa + 1) * 2048 + tok) * 16, 0, SC1);
    const u32x2 e = __builtin_amdgcn_raw_buffer_load_b64(p.rws, base + (ob * 2048 + tok) * 16 + odd * 8, 0, SC1);
#pragma unroll
    for (int k = 0; k < 8; ++k) { dst[k] = unpk(a, k); dst[8 + k] = unpk(c, k); }
    dst[16] = bf_lo(e[0]); dst[17] = bf_hi(e[0]); dst[18] = bf_lo(e[1]); dst[19] = bf_hi(e[1]);
  };
  row20(WS_Q, tok0 + r, q);
#pragma unroll
  for (int d = 0; d < 20; ++d) acc[d] = 0.f;
#pragma unroll 1
  for (int g = 0; g < 256; ++g) {
    float pr[4], gm = -INFINITY;
#pragma unroll
    for (int j = 0; j < 4; ++j) {
      float k[20]; row20(WS_K, g * 8 + 4 * hh + j, k);
      float s = 0.f;
#pragma unroll
      for (int d = 0; d < 20; ++d) s = fmaf(q[d], k[d], s);
      pr[j] = s; gm = fmaxf(gm, s);
    }
    const float mn = fmaxf(mx, gm), sc = exp2f(mx - mn);
    mx = mn; l *= sc;
#pragma unroll
    for (int j = 0; j < 4; ++j) { pr[j] = rbf(exp2f(pr[j] - mn)); l += pr[j]; }
#pragma unroll
    for (int d = 0; d < 20; ++d) {
      const int dd = odd ? (d < 16 ? d + 4 : d - 16) : d;
      const u32x2 v = __builtin_amdgcn_raw_buffer_load_b64(p.rws, WS_V + (((g >> 1) * 2 + hh) * 640 + h * 20 + dd) * 16 + (g & 1) * 8, 0, SC1);
      acc[d] = fmaf(pr[3], bf_hi(v[1]), fmaf(pr[2], bf_lo(v[1]), fmaf(pr[1], bf_hi(v[0]), fmaf(pr[0], bf_lo(v[0]), acc[d] * sc))));
    }
  }
  const float mn = fmaxf(mx, swap32(mx)), sc = exp2f(mx - mn);
  l *= sc; l += swap32(l);
#pragma unroll
  for (int d = 0; d < 20; ++d) { acc[d] *= sc; acc[d] += swap32(acc[d]); }
#pragma unroll
  for (int i = 0; i < 8; ++i) o[i >> 2][i & 3] = hh ? acc[(i & 3) + 8 * (i >> 2) + 4] : acc[(i & 3) + 8 * (i >> 2)];
#pragma unroll
  for (int i = 8; i < 12; ++i) o[2][i - 8] = acc[8 + i];
  lout = l;
}

#define ATTN_CLOB "v0","v1","v2","v3","v4","v5","v6","v7","v8","v9",C10(1),C10(2),C10(3),C10(4),C10(5),"v60","v61","v62","v63", \
  "v130","v131","v136","v137","v138","v139","v140","v141","v142","v143","v148","v149",C10(15),"v160","v161","v162","v163","v189","vcc","scc","memory"

DI int head_col(int sec, int h, int last) { return sec == 2 ? h * 20 + last * 19 : (h >> 1) * 40 + (h & 1) * 16 + last * 23; }

DI u32x2 o_addr(const Ctx& p, int h, int tok) {
  const int r = p.lane & 31, hh = p.lane >> 5, base = WS_O + (((tok + r) >> 4) * 20 + (h >> 3) * 5) * 1024, tl = r & 15;
  return (u32x2){(unsigned)(base + (h & 7) * 512 + hh * 256 + tl * 16), hh ? WS_END : base + 4096 + (h & 7) * 128 + tl * 8};
}

DI void attention(const Ctx& p, int h, int b) {
  const int lane = p.lane, wid = p.wid, qg = wid >> 1, kp = wid & 1, r = lane & 31, hh = lane >> 5;
  f32x16 o0, o1, o2, o3; u32x4 z0, z1;
  asm volatile(ATTN_ZERO_C : "={v[64:79]}"(o0), "={v[80:95]}"(o1), "={v[96:111]}"(o2), "={v[112:127]}"(o3), "={v[132:135]}"(z0), "={v[144:147]}"(z1));
  const unsigned aK = kp * 2048 + lane * 16, aKB = 4096 + kp * 512 + r * 16;
  const unsigned aV = r <= 20 ? 5120 + (kp * 8 + hh) * 336 + r * 16 : L_OOB;
  const unsigned aD = lane == 0 ? L_KV : L_OOB, one = 1, inc = lane == 0 ? 0x10000 : 0;
  unsigned need = inc;
  u32x2 oa = o_addr(p, h, b * 256 + wid * 64);
  unsigned ex = (qg ? 0 : L_EX) + kp * 6144 + lane * 16, xw = lane == 4 ? L_XF + (wid ^ 1) * 4 : L_XF + 32 + (lane & 3) * 4;
  PIN(oa); PIN(ex); PIN(xw);
  TMARK(5);
  lds_wait1<0>(L_QF + qg * 4, ~0u, 0x10000);
  u32x16 qa; u32x4 qb;
#pragma unroll
  for (int j = 0; j < 4; ++j) {
    const u32x4 t = *(const LAS u32x4*)(smem + L_EX + (qg * 4 + (j ^ (kp * 2))) * 1024 + lane * 16);
    qa[4 * j] = t[0]; qa[4 * j + 1] = t[1]; qa[4 * j + 2] = t[2]; qa[4 * j + 3] = t[3];
  }
#pragma unroll
  for (int j = 0; j < 2; ++j) {
    const u32x2 t = *(const LAS u32x2*)(smem + L_EX + 8192 + (qg * 128 + (j ^ kp) * 64 + lane) * 8);
    qb[2 * j] = t[0]; qb[2 * j + 1] = t[1];
  }
  PIN(qa); PIN(qb);
  TMARK(6);
  lds_wait<0>(L_KV + 8 + (lane & 1) * 4, ~0u, 0x10000);
  TMARK(7); CMARK(14);
  int it = 4, spin = SPIN_CAP, msk, sm = 0;
  u32x2 done;
  asm volatile(ATTN_ASM_C : "+{v[64:79]}"(o0), "+{v[80:95]}"(o1), "+{v[96:111]}"(o2), "+{v[112:127]}"(o3), "+{v[132:135]}"(z0), "+{v[144:147]}"(z1),
                 "+{v190}"(need), "={v[128:129]}"(done), [it] "+s"(it), [spin] "+s"(spin), [msk] "=&s"(msk)
               : "{v[164:179]}"(qa), "{v[180:183]}"(qb), "{v184}"(aK), "{v185}"(aKB), "{v186}"(aV), "{v187}"(aD), "{v188}"(one), "{v191}"(inc)
               : ATTN_CLOB);
  TMARK(8); CMARK(15);
  __builtin_amdgcn_raw_buffer_store_b64(p.vtag, p.rws, p.v0, WS_F3 + ((b * 4 + wid) * 32 + h) * 128, SC1);
  if (__builtin_expect(qg && (__builtin_amdgcn_readfirstlane(done[0] & done[1]) & 16) == 0, 0)) lds_wait<1>(L_KV + (lane & 1) * 4, 0xffff, 16);
#pragma unroll
  for (int g = 0; g < 3; ++g) {
    *(LAS f32x4*)(uintptr_t)(ex + g * 1024) = (f32x4){o2[4 * g], o2[4 * g + 1], o2[4 * g + 2], o2[4 * g + 3]};
    *(LAS f32x4*)(uintptr_t)(ex + 3072 + g * 1024) = (f32x4){o3[4 * g], o3[4 * g + 1], o3[4 * g + 2], o3[4 * g + 3]};
  }
  fence();
  lds_st(L_XF + wid * 4, 1);
  lds_wait<0>(xw, ~0u, 1);
  TMARK(9);
  f32x4 o[2][3], chk = {};
  float l[2];
  const unsigned exr = ex + (kp ? -6144 : 6144);
#pragma unroll
  for (int j = 0; j < 2; ++j)
#pragma unroll
    for (int g = 0; g < 3; ++g) o[j][g] = *(LAS f32x4*)(uintptr_t)(exr + j * 3072 + g * 1024);
  ISSUED();
#pragma unroll
  for (int j = 0; j < 2; ++j) {
    const f32x16& a = j ? o1 : o0;
    o[j][0] += __builtin_shufflevector(a, a, 0, 1, 2, 3); o[j][1] += __builtin_shufflevector(a, a, 4, 5, 6, 7); o[j][2] += __builtin_shufflevector(a, a, 8, 9, 10, 11);
#pragma unroll
    for (int g = 0; g < 3; ++g) chk += o[j][g] * 0.0f;
    const auto sw = __builtin_amdgcn_permlane32_swap(__float_as_uint(o[j][2][0]), __float_as_uint(o[j][2][0]), false, false);
    l[j] = __uint_as_float(sw[1]);
  }
  auto put = [&](int j, const f32x4 (&t)[3], float lj) {
    const float inv = __builtin_amdgcn_rcpf(lj);
    const f32x4 u = t[0] * inv, v = t[1] * inv, w = t[2] * inv;
    const auto p0 = __builtin_amdgcn_permlane32_swap(cvt_pk(u[0], u[1]), cvt_pk(v[0], v[1]), false, false);
    const auto p1 = __builtin_amdgcn_permlane32_swap(cvt_pk(u[2], u[3]), cvt_pk(v[2], v[3]), false, false);
    st128(p.rws, (u32x4){nomark(p0[0]), nomark(p1[0]), nomark(p0[1]), nomark(p1[1])}, oa[0] + j * 40960);
    st64(p.rws, (u32x2){nomark(cvt_pk(w[0], w[1])), nomark(cvt_pk(w[2], w[3]))}, oa[1] + j * 40960);
  };
  if (__builtin_expect(__any(!(fminf(l[0], l[1]) + ((chk[0] + chk[1]) + (chk[2] + chk[3])) >= 0x1p-90f && fmaxf(l[0], l[1]) <= 0x1p120f)), 0)) {
#pragma unroll 1
    for (int j = 0; j < 2; ++j) {
      f32x4 e[3]; float le;
      attn_exact(p, h, b * 256 + wid * 64 + j * 32, e, le);
      put(j, e, le);
    }
  } else {
    put(0, o[0], l[0]); put(1, o[1], l[1]);
  }
  TMARK(10);
}

struct Stage { unsigned vka, vkb, vv[6], vf, lr, inc; unsigned long long on[6]; };
DI void stage_init(const Ctx& p, int h, int hw, Stage& s) {
  const int lane = p.lane, odd = h & 1, oa = (h >> 1) * 5 + 3 * odd, ob = (h >> 1) * 5 + 2;
  s.vka = WS_K + ((oa + (lane >> 5)) * 2048 + (lane & 31)) * 16;
  s.vkb = WS_K + (ob * 2048 + ((lane >> 1) & 1) * 32 + (lane >> 2)) * 16 + odd * 8 + (lane & 1) * 4;
#pragma unroll
  for (int i = 0; i < 6; ++i) {
    const int e = i * 64 + lane, G = e / 21, r = e - G * 21;
    s.vv[i] = WS_V + (G * 640 + h * 20 + (odd ? (r < 16 ? r + 4 : r - 16) : r)) * 16;
    s.on[i] = __ballot(r < 20 && G < 16);
    PIN(s.vv[i]);
  }
  const int sec = 1 + ((lane >> 2) & 1);
  s.vf = WS_F1 + ((sec * 10 + (head_col(sec, h, lane & 1) >> 6)) * 2 + ((lane >> 1) & 1)) * 128;
  s.lr = lane == 0 ? L_KV + ((hw + 2) & 3) * 4 : L_OOB; s.inc = 0x10000;
  PIN(s.vka); PIN(s.vkb); PIN(s.vf); PIN(s.lr); PIN(s.inc);
}
DI void stage(const Ctx& p, int hw, int i, const Stage& s, bool seen = false) {
  const int c = hw + 4 * i;
  if (!seen) flag_wait<1>(p, s.vf, c * 60 * 128);
  if (i) lds_wait1<8>(L_KV + hw * 4, 0xffff, 4 * i);
  else TMARK(19);
#pragma unroll
  for (int t = 0; t < 4; ++t) {
    dma16<1>(p.rws, s.vka, (c * 128 + t * 32) * 16, hw * SL + t * 1024);
    dma4(p.rws, s.vkb, (c * 128 + (t >> 1) * 64 + (t & 1) * 16) * 16, hw * SL + 4096 + t * 256);
  }
#pragma unroll
  for (int t = 0; t < 6; ++t) dma16_masked(p.rws, s.vv[t], c * 16 * 10240, hw * SL + 5120 + t * 1024, s.on[t]);
  wait_vm<0>();
  asm volatile("ds_add_u32 %0, %1" ::"v"(s.lr), "v"(s.inc) : "memory");
}

DI bf16x8 tr_frag(const LAS char* a, int second) {
  const bf16x4 lo = __builtin_amdgcn_ds_read_tr16_b64_v4i16((LAS bf16x4*)a), hi = __builtin_amdgcn_ds_read_tr16_b64_v4i16((LAS bf16x4*)(a + second));
  return (bf16x8){lo[0], lo[1], lo[2], lo[3], hi[0], hi[1], hi[2], hi[3]};
}
struct UpAddr { unsigned w, fs, x, o, l; };
DI void ffn_up_init(const Ctx& p, int job, unsigned scr, UpAddr& u) {
  const int lane = p.lane, g = lane >> 4, t = lane & 15;
  u.w = (lane >> 3) * 3072 + job / 10 * 128 + (lane & 7) * 16;
  u.fs = WS_FS + (lane & 3) * 128;
  u.x = t < 8 ? WS_XS + (job % 10 * 64 + g * 8 + t) * 16 : WS_END;
  u.o = t < 8 ? WS_PU + job * 4096 + t * 64 + g * 16 : WS_END;
  u.l = scr + (g * 8 + (t >> 2)) * 128 + (lane & 3) * 8;
  PIN(u.w); PIN(u.fs); PIN(u.x); PIN(u.o); PIN(u.l);
}
DI void ffn_up(const Ctx& p, int job, unsigned scr, const UpAddr& u) {
  const rsrc_t rg = mk_rsrc(p.wg, D * F * 2), ru = mk_rsrc(p.wu, D * F * 2);
#pragma unroll
  for (int i = 0; i < 8; ++i) {
    dma16<2>(rg, u.w, (job % 10 * 64 + i * 8) * 3072, scr + i * 1024);
    dma16<2>(ru, u.w, (job % 10 * 64 + i * 8) * 3072, scr + 8192 + i * 1024);
  }
  flag_wait<8>(p, u.fs);
  u32x4 xb[2];
#pragma unroll
  for (int k = 0; k < 2; ++k) xb[k] = ld128(p.rws, u.x, k * 512, SC1);
  wait_vm<0>();
  const LAS char* a = (const LAS char*)(uintptr_t)u.l;
#pragma unroll
  for (int c = 0; c < 8; ++c) {
    f32x4 acc = {};
#pragma unroll
    for (int k = 0; k < 2; ++k)
      acc = __builtin_amdgcn_mfma_f32_16x16x32_bf16(tr_frag(a + (c >> 2) * 8192 + k * 4096 + (c & 3) * 32, 512), __builtin_bit_cast(bf16x8, xb[k]), acc, 0, 0, 0);
    st128(p.rws, __builtin_bit_cast(u32x4, acc), u.o + c * 512);
  }
  flag_set(p, WS_FU + job * 128);
}
DI void ffn_act(const Ctx& p, int job) {
  const int lane = p.lane;
  flag_wait<4>(p, WS_FU + ((job >> 2) * 10 + min(lane, 9)) * 128);
  u32x4 v[10];
#pragma unroll
  for (int k = 0; k < 10; ++k) v[k] = ld128(p.rws, WS_PU + (lane >> 5) * 2048 + (lane & 31) * 16, ((job >> 2) * 10 + k) * 4096 + (job & 3) * 512, SC1);
  ISSUED();
  f32x4 s = __builtin_bit_cast(f32x4, v[0]);
#pragma unroll
  for (int k = 1; k < 10; ++k) s += __builtin_bit_cast(f32x4, v[k]);
  float hv[4];
#pragma unroll
  for (int k = 0; k < 4; ++k) { const float gt = rbf(s[k]); hv[k] = rbf(gt * sigmoid(gt)) * rbf(swap32(s[k])); }
  if (lane < 32) st64(p.rws, (u32x2){cvt_pk(hv[0], hv[1]), cvt_pk(hv[2], hv[3])}, WS_H + ((job * 2 + ((lane >> 1) & 1)) * 8 + (lane >> 2)) * 16 + (lane & 1) * 8);
  flag_set(p, WS_FH + job * 128);
}
DI void ffn_down_fetch(const Ctx& p, int ct, int kq, unsigned scr) {
  const rsrc_t rd = mk_rsrc(p.wd, D * F * 2);
#pragma unroll
  for (int i = 0; i < 12; ++i) dma16(rd, (p.lane >> 1) * 1280 + ct * 32 + (p.lane & 1) * 16, (kq * 384 + i * 32) * 1280, scr + i * 1024);
}
DI void ffn_down(const Ctx& p, int ct, int kq, unsigned scr) {
  const int lane = p.lane, g = lane >> 4, t = lane & 15;
  flag_wait<4>(p, WS_FH + (kq * 24 + min(lane, 23)) * 128);
  u32x4 hb[12];
#pragma unroll
  for (int k = 0; k < 12; ++k) hb[k] = ld128(p.rws, t < 8 ? WS_H + (kq * 48 * 8 + g * 8 + t) * 16 : WS_END, k * 512, SC1);
  wait_vm<0>();
  const LAS char* a = (const LAS char*)smem + scr + (g * 8 + (t >> 2)) * 32 + (lane & 3) * 8;
  f32x4 acc = {};
#pragma unroll
  for (int k = 0; k < 12; ++k) {
    acc = __builtin_amdgcn_mfma_f32_16x16x32_bf16(tr_frag(a + k * 1024, 128), __builtin_bit_cast(bf16x8, hb[k]), acc, 0, 0, 0);
  }
  if (t < 8) st128(p.rws, __builtin_bit_cast(u32x4, acc), WS_Y + ((kq * 8 + t) * 640 + ct * 16 + g * 4) * 4);
  flag_set(p, WS_FY + (ct * 4 + kq) * 128);
}

struct Routed { u32x4 y[5][4]; float w; };
DI void routed_load(const Ctx& p, int row0, int ct, int hw, const u32x2& sel, unsigned hits, Routed& r) {
  flag_wait<4>(p, WS_FY + (ct * 20 + min(p.lane, 19)) * 128);
  unsigned v = WS_END; r.w = 0.f;
#pragma unroll 1
  for (; hits; hits &= hits - 1) {
    const int j = __builtin_ctz(hits);
    const bool hit = (int)__builtin_amdgcn_readlane(sel[0], j) - row0 - hw * 16 == (p.lane & 15);
    v = hit ? WS_Y + (j * 640 + ct * 80) * 4 + (p.lane & 48) : v;
    r.w = hit ? __uint_as_float(__builtin_amdgcn_readlane(sel[1], j)) : r.w;
  }
#pragma unroll
  for (int i = 0; i < 5; ++i)
#pragma unroll
    for (int kq = 0; kq < 4; ++kq) r.y[i][kq] = ld128(p.rws, v, kq * 20480 + i * 64, SC1);
  ISSUED();
}
struct GateF { f32x4 x[5], g[5]; };
template <bool ROUTED> DI void gate_eval(const Gate& gt, const Routed& r, GateF& f) {
  const float mb = bf_lo(gt.st[0]), rs = __builtin_amdgcn_rcpf(bf_hi(gt.st[0]));
#pragma unroll
  for (int i = 0; i < 5; ++i) {
    f.x[i] = (f32x4){bf_lo(gt.x[i][0]), bf_hi(gt.x[i][0]), bf_lo(gt.x[i][1]), bf_hi(gt.x[i][1])};
    f32x4 y;
    if (ROUTED) y = (__builtin_bit_cast(f32x4, r.y[i][0]) + __builtin_bit_cast(f32x4, r.y[i][1])) + (__builtin_bit_cast(f32x4, r.y[i][2]) + __builtin_bit_cast(f32x4, r.y[i][3]));
#pragma unroll
    for (int k = 0; k < 4; k += 2) {
      const unsigned c = cvt_pk(f.x[i][k] - mb, f.x[i][k + 1] - mb);
      unsigned z = cvt_pk(bf_lo(c) * rs, bf_hi(c) * rs);
      if (ROUTED) {
        const unsigned yb = cvt_pk(y[k], y[k + 1]), u = cvt_pk(bf_lo(yb) * r.w, bf_hi(yb) * r.w);
        z = cvt_pk(bf_lo(z) + bf_lo(u), bf_hi(z) + bf_hi(u));
      }
      const unsigned g = cvt_pk(bf_lo(z) * sigmoid(bf_lo(z)), bf_hi(z) * sigmoid(bf_hi(z)));
      f.g[i][k] = bf_lo(g); f.g[i][k + 1] = bf_hi(g);
    }
    PIN(f.x[i]); PIN(f.g[i]);
  }
}

DI bool stage_q(const Ctx& p, int h, int b, int half, const Stage& s) {
  const int lane = p.lane, odd = h & 1;
  const unsigned vf = lane < 8 ? s.vf + (2 + half) * 60 * 128 : WS_F1 + (((b * 2 + half) * 30 + (head_col(0, h, lane & 1) >> 6)) * 2 + ((lane >> 1) & 1)) * 128;
  unsigned long long miss = 0;
#pragma unroll 1
  for (int i = 0; i < SPIN_CAP && ((miss = flag_misses(p, vf, 0)) >> 8); ++i) __builtin_amdgcn_s_sleep(1);
  fence();
  const unsigned va = WS_Q + (((h >> 1) * 5 + 3 * odd + (lane >> 5)) * 2048 + b * 256 + half * 128 + (lane & 31)) * 16;
  const unsigned vb = WS_Q + (((h >> 1) * 5 + 2) * 2048 + b * 256 + half * 128 + (lane >> 1)) * 16 + odd * 8 + (lane & 1) * 4;
#pragma unroll
  for (int t = 0; t < 4; ++t) {
    dma16<1>(p.rws, va, t * 512, L_EX + (half * 4 + t) * 1024);
    dma4(p.rws, vb, t * 512, L_EX + 8192 + half * 1024 + t * 256);
  }
  wait_vm<0>();
  lds_st(L_QF + half * 4, 0x10000);
  return 1 && !miss;
}

struct WFrag { u32x32 w0, w1, w2; u32x4 w3; unsigned wb; };
#define W_OUT "={v[60:91]}"(w.w0), "={v[92:123]}"(w.w1), "={v[124:155]}"(w.w2), "={v[156:159]}"(w.w3)
#define W_INOUT "+{v[60:91]}"(w.w0), "+{v[92:123]}"(w.w1), "+{v[124:155]}"(w.w2), "+{v[156:159]}"(w.w3)
DI void out_pre(const Ctx& p, int g0, WFrag& w) {
  w.wb = L_WO + g0 * 25600 + ((p.lane >> 4) * 4 + ((p.lane & 15) >> 2)) * 160 + (p.lane & 3) * 8;
  asm volatile(OPROJ_PRE : W_OUT : "{v160}"(w.wb) : "memory");
}

DI void attn_helper(const Ctx& p, int h, int b, GateF& gf, WFrag& wf) {
  const int hw = p.wid - 4, lane = p.lane, row0 = b * 256 + (h >> 3) * 64, ct = h & 7;
  const int xcd = p.bid & 7, idx = p.bid >> 3;
  const unsigned scr = L_WO + hw * 25600;
  const bool up = !(0 & 1) && hw == 3 && idx < 30, act = !(0 & 1) && hw == 1 && idx < 12, down = !(0 & 2) && hw == 2 && idx >= 12;
  const int upjob = xcd * 30 + idx, dct = xcd * 5 + (idx - 12) % 5, dkq = (idx - 12) / 5;
  Stage s;
  TMARK(16);
  stage_init(p, h, hw, s);
  unsigned vw[5], vs[5];
#pragma unroll
  for (int i = 0; i < 5; ++i) {
    const int e = i * 64 + lane, pos = e / 10, seg = e - pos * 10, g = (pos >> 2) & 3, j = (pos >> 4) * 4 + (pos & 3);
    vw[i] = ((g + (g >> 1)) * 8 + j) * 1280 + ct * 160 + seg * 16;
    vs[i] = (g * 40 + 16 + j) * 1280 + ct * 160 + seg * 16;
    PIN(vw[i]); PIN(vs[i]);
  }
  UpAddr ua;
  ffn_up_init(p, upjob, scr, ua);
  u32x2 oa = o_addr(p, h, b * 256 + hw * 64), oa2 = oa + 40960;
  u32x4 mk = {MARK, MARK, MARK, MARK};
  const int tok = row0 + hw * 16 + (lane & 15);
  unsigned vsel = WS_SEL + (lane & 7) * 8, vf2 = WS_F2 + ((b * 4 + (h >> 3)) * 32 + (lane & 31)) * 128;
  unsigned vst = WS_STG + tok * 16, vx = tok * 1280 + ct * 160 + (lane >> 4) * 8, vo = tok * 1280 + ct * 160 + (lane >> 4) * 16;
  PIN(oa); PIN(oa2); PIN(mk); PIN(vsel); PIN(vf2); PIN(vst); PIN(vx); PIN(vo);
  if (lane < 16) *(LAS u32x4*)(smem + hw * SL + 5120 + lane * 336 + 320) = (u32x4){0x3f803f80u, 0x3f803f80u, 0x3f803f80u, 0x3f803f80u};
  __builtin_amdgcn_s_setprio(3);
  TMARK(17);
  if (60) __builtin_amdgcn_s_sleep(60);
  bool seen = false;
  if (hw >= 2) seen = stage_q(p, h, b, hw - 2, s);
  TMARK(18);
  stage(p, hw, 0, s, seen);
  TMARK(20);
  if (up) ffn_up(p, upjob, scr, ua);
  TMARK(21);
  stage(p, hw, 1, s);
  TMARK(22);
  st128(p.rws, mk, oa[0]); st64(p.rws, (u32x2){mk[0], mk[1]}, oa[1]); st128(p.rws, mk, oa2[0]); st64(p.rws, (u32x2){mk[0], mk[1]}, oa2[1]);
  flag_set(p, WS_F2 + ((b * 4 + hw) * 32 + h) * 128);
  if (act) ffn_act(p, xcd * 12 + idx);
  if (down) ffn_down_fetch(p, dct, dkq, scr);
  flag_wait<8>(p, ua.fs);
  const u32x2 sel = __builtin_amdgcn_raw_buffer_load_b64(p.rws, vsel, 0, SC1);
  TMARK(23);
  stage(p, hw, 2, s);
  TMARK(24);
  if (down) ffn_down(p, dct, dkq, scr);
  TMARK(25);
  const rsrc_t rw = mk_rsrc(p.wo, D * D * 2);
  wait_lds();
#pragma unroll 1
  for (int step = 0; step < 2; ++step) {
    if ((step == 0) == (1 && hw == 2)) stage(p, hw, 3, s);
    else {
#pragma unroll 1
      for (int k = 0; k < 4; ++k) {
#pragma unroll
        for (int i = 0; i < 5; ++i) dma16(rw, vw[i], (hw * 160 + k * 40) * 1280, scr + k * 5120 + i * 1024);
        wait_vm<2>();
      }
#pragma unroll
      for (int i = 0; i < 5; ++i) dma16(rw, vs[i], hw * 160 * 1280, scr + 4 * 5120 + i * 1024);
    }
  }
  TMARK(27);
  Gate gt;
  gt.st = ld128(p.rws, vst, 0, SC1);
#pragma unroll
  for (int i = 0; i < 5; ++i) gt.x[i] = __builtin_amdgcn_raw_buffer_load_b64(mk_rsrc(p.x, MAT), vx, i * 32, 0);
  const rsrc_t rd = mk_rsrc(p.out, MAT);
  __builtin_amdgcn_raw_buffer_store_b128(mk, rd, vo, 0, 16);
  __builtin_amdgcn_raw_buffer_store_b128(mk, rd, vo + 64, 0, 16);
  __builtin_amdgcn_raw_buffer_store_b64((u32x2){mk[0], mk[1]}, rd, vx + 128, 0, 16);
  wait_vm<0>();
  flag_wait<8>(p, vf2);
  wait_lds();
  lds_st(L_XF + 32 + hw * 4, 1);
  TMARK(28);
  Routed rt;
  const unsigned hits = (unsigned)__ballot((unsigned)(sel[0] - row0 - hw * 16) < 16u) & 0xff;
  if (__builtin_expect(hits != 0, 0)) {
    routed_load(p, row0, ct, hw, sel, hits, rt);
    __builtin_amdgcn_s_setprio(0);
    lds_wait1<2>(L_XF + hw * 4, ~0u, 1);
    gate_eval<true>(gt, rt, gf);
    lds_wait<1>(L_XF + 32 + (lane & 3) * 4, ~0u, 1);
    out_pre(p, 0, wf);
  } else {
    __builtin_amdgcn_s_setprio(0);
    lds_wait<8>(L_XF + 32 + (lane & 3) * 4, ~0u, 1);
    out_pre(p, 0, wf);
    lds_wait1<2>(L_XF + hw * 4, ~0u, 1);
    TMARK(29);
    gate_eval<false>(gt, rt, gf);
  }
  TMARK(30);
}

struct OFrag { u32x4 a[5]; };
DI void o_load(const Ctx& p, unsigned so, OFrag& f) {
#pragma unroll
  for (int k = 0; k < 4; ++k) f.a[k] = ld128(p.rws, WS_O + p.lane * 16, so + k * 1024, SC1);
  const unsigned v = WS_O + 4096 + (p.lane >> 4) * 256 + (p.lane & 15) * 8;
  const u32x2 lo = __builtin_amdgcn_raw_buffer_load_b64(p.rws, v, so, SC1), hi = __builtin_amdgcn_raw_buffer_load_b64(p.rws, v + 128, so, SC1);
  f.a[4] = (u32x4){lo[0], lo[1], hi[0], hi[1]};
  ISSUED();
}
DI void o_valid(const Ctx& p, unsigned so, OFrag& f) {
  auto marked = [&]() { return __any(max(max(max(umax4(f.a[0]), umax4(f.a[1])), max(umax4(f.a[2]), umax4(f.a[3]))), umax4(f.a[4])) == MARK); };
  if (marked()) {
#pragma unroll 1
    for (int i = 0; i < SPIN_CAP; ++i) {
      fence();
      o_load(p, so, f);
      if (!marked()) break;
    }
  }
}
DI void out_proj(const Ctx& p, int row0, int mt, int g0, WFrag& w, f32x4 (&sum)[5]) {
  const int lane = p.lane;
  const unsigned so = (((row0 >> 4) + mt) * 20 + g0 * 5) * 1024, wb = w.wb;
#define A_IN(f) "{v[0:3]}"(f.a[0]), "{v[4:7]}"(f.a[1]), "{v[8:11]}"(f.a[2]), "{v[12:15]}"(f.a[3]), "{v[16:19]}"(f.a[4])
  flag_wait<1>(p, WS_F3 + (lane & 15) * 128, ((row0 >> 6) * 32 + g0 * 8) * 128);
  TMARK(31);
  OFrag fa, fb;
  o_load(p, so, fa);
  o_load(p, so + 5120, fb);
  o_valid(p, so, fa);
  asm volatile(OPROJ_FIRST : "={v[20:23]}"(sum[0]), "={v[24:27]}"(sum[1]), "={v[28:31]}"(sum[2]), "={v[32:35]}"(sum[3]), "={v[36:39]}"(sum[4]), W_INOUT
               : A_IN(fa), "{v160}"(wb) : "memory");
  TMARK(32);
  o_valid(p, so + 5120, fb);
  asm volatile(OPROJ_SECOND : "+{v[20:23]}"(sum[0]), "+{v[24:27]}"(sum[1]), "+{v[28:31]}"(sum[2]), "+{v[32:35]}"(sum[3]), "+{v[36:39]}"(sum[4]), W_INOUT
               : "{v[40:43]}"(fb.a[0]), "{v[44:47]}"(fb.a[1]), "{v[48:51]}"(fb.a[2]), "{v[52:55]}"(fb.a[3]), "{v[56:59]}"(fb.a[4]) : "memory");
}
DI void out_store(const Ctx& p, int row0, int ct, const f32x4 (&acc)[5], const GateF& gt) {
  const int hw = p.wid - 4, g = p.lane >> 4;
  const rsrc_t rd = mk_rsrc(p.out, MAT);
  const unsigned row = (row0 + hw * 16 + (p.lane & 15)) * 1280 + ct * 160;
  f32x4 part[5];
#pragma unroll 1
  for (int i = 0; i < SPIN_CAP; ++i) {
    const unsigned f = lds_ld(L_XF + p.wid * 4);
#pragma unroll
    for (int k = 0; k < 5; ++k) part[k] = *(volatile LAS f32x4*)(smem + L_ACC + (hw * 5 + k) * 1024 + p.lane * 16);
    if (__builtin_amdgcn_readfirstlane(f)) break;
  }
  u32x2 o[5];
#pragma unroll
  for (int i = 0; i < 5; ++i) {
    const f32x4 v = acc[i] + part[i];
#pragma unroll
    for (int k = 0; k < 2; ++k) {
      const unsigned s = cvt_pk(v[2 * k], v[2 * k + 1]);
      const unsigned t = cvt_pk(bf_lo(s) * gt.g[i][2 * k], bf_hi(s) * gt.g[i][2 * k + 1]);
      o[i][k] = cvt_pk(gt.x[i][2 * k] + bf_lo(t), gt.x[i][2 * k + 1] + bf_hi(t));
    }
    if (i & 1) {
      const auto s0 = __builtin_amdgcn_permlane16_swap(o[i - 1][0], o[i][0], false, false), s1 = __builtin_amdgcn_permlane16_swap(o[i - 1][1], o[i][1], false, false);
      __builtin_amdgcn_raw_buffer_store_b128((u32x4){s0[0], s1[0], s0[1], s1[1]}, rd, row + (i - 1 + (g & 1)) * 32 + (g >> 1) * 16, 0, 16);
    }
  }
  __builtin_amdgcn_raw_buffer_store_b64(o[4], rd, row + 128 + g * 8, 0, 16);
}

__global__ void __launch_bounds__(512, 2) k_block(const void* x, const void* wqkv, const void* wo, const void* wr, const void* wg, const void* wu,
                                                  const void* wd, char* ws, void* out) {
  ISSUED();
  Ctx p;
  p.x = x; p.wqkv = wqkv; p.wo = wo; p.wr = wr; p.wg = wg; p.wu = wu; p.wd = wd; p.ws = ws; p.out = out;
  p.bid = blockIdx.x;
  p.wid = __builtin_amdgcn_readfirstlane(threadIdx.x >> 6); p.lane = threadIdx.x & 63;
  TMARK(63);
  const int xcd = p.bid & 7, idx = p.bid >> 3;
  const int m = (idx & 3) * 4 + (xcd >> 1), n = ((idx >> 2) * 2 + (xcd & 1)) ^ ((idx >> 1) & ((idx + 4) >> 5) & 1);
  const int h = xcd * 4 + (idx & 3), b = idx >> 2;
  const int row0 = b * 256 + (h >> 3) * 64, ct = h & 7;
  const int cn = ((xcd >> 1) * 2 + (idx & 1)) * 2 + (xcd & 1);
  Feed feed;
  feed_init(p, m, n, __builtin_amdgcn_readfirstlane(((idx ^ (idx >> 2)) & 1) * 5), feed);
  TMARK(0);
  RowData rd;
  if (p.wid < 4) row_load(p, m * 128 + n * 8 + p.wid * 2, rd);
  qkv_issue<2>(feed, p.wid & 3, (p.wid >> 2) * 2, 0);
  p.rws = mk_rsrc(ws, WS_END);
  unsigned long long pc; unsigned code_bytes;
  asm volatile("s_getpc_b64 %0\n.Lk_block_pc:\n\ts_mov_b32 %1, .Lk_block_end0-.Lk_block_pc\n\ts_max_i32 %1, %1, .Lk_block_end1-.Lk_block_pc" : "=s"(pc), "=s"(code_bytes)::"scc");
  const rsrc_t rcode = mk_rsrc((const void*)(pc & ~127ull), code_bytes);
  if (p.wid >= 4) {
    if (n < 15) qkv_dma(p, feed, m, n);
    else if (cn < 15) col_sums(p, cn);
    else { bar(); read_tags(p); }
  } else {
    if (p.wid == 1 && p.lane < 20) lds_st(L_KV + p.lane * 4, 0);
    row_stats(p, m * 128 + n * 8 + p.wid * 2, rd);
    if (0 && p.wid == 3) touch_pages(p);
    asm volatile("s_waitcnt vmcnt(1) lgkmcnt(0)" ::: "memory");
    TMARK(1);
    if (n == 15) { bar(); if (cn < 15) bar(); }
    else if (col_half(n, p.wid & 1) >= 20) qkv_mfma<true>(p, m, col_half(n, p.wid & 1));
    else qkv_mfma<false>(p, m, col_half(n, p.wid & 1));
  }
  if (n == 15 && cn == 15) route(p);
  f32x4 acc[5];
  if (p.wid < 4) {
    attention(p, h, b);
    lds_wait<0>(L_KV + 8 + (p.lane & 1) * 4, 0xffff, 16);
    WFrag wf;
    out_pre(p, 2, wf);
    out_proj(p, row0, p.wid, 2, wf, acc);
    TMARK(33);
#pragma unroll
    for (int i = 0; i < 5; ++i) *(LAS f32x4*)(smem + L_ACC + (p.wid * 5 + i) * 1024 + p.lane * 16) = acc[i];
    fence();
    lds_st(L_XF + 16 + p.wid * 4, 1);
    asm volatile(".Lk_block_end0:");
  } else {
    if (2 == 2 && p.wid == 4) dma4(rcode, (p.lane * 32 + idx) * 128, 0, L_WO);
    GateF gf;
    WFrag wf;
    attn_helper(p, h, b, gf, wf);
    out_proj(p, row0, p.wid - 4, 0, wf, acc);
    TMARK(33);
    out_store(p, row0, ct, acc, gf);
    asm volatile(".Lk_block_end1:");
  }
  TMARK(34);
  TMARK(35);
  TDUMP();
}

extern "C" void kernel_launch(void* const* d_in, const int* in_sizes, int n_in, void* d_out, int out_size, void* d_ws, size_t ws_size, hipStream_t stream) {
  (void)hipFuncSetAttribute((const void*)k_block, hipFuncAttributeMaxDynamicSharedMemorySize, LDS_BYTES);
  k_block<<<256, 512, LDS_BYTES, stream>>>(d_in[0], d_in[1], d_in[2], d_in[4], d_in[5], d_in[6], d_in[7], (char*)d_ws, d_out);
}
```
